```python
import torch

def _softmax(x, axis=-1):
    return torch.softmax(x, dim=axis)

def _gelu(x):
    return torch.nn.functional.gelu(x, approximate='tanh')

def _silu(x):
    return torch.nn.functional.silu(x)

def _sigmoid(x):
    return torch.sigmoid(x)

def _layernorm(x, axis=-1, eps=1e-05):
    m = x.mean(dim=axis, keepdim=True)
    v = x.var(dim=axis, keepdim=True, unbiased=False)
    return (x - m) / torch.sqrt(v + eps)

def _scatter_add(tgt, idx, src, axis=0):
    return tgt.clone().index_add_(axis, idx, src)

def _segment_sum(src, seg, n):
    out = torch.zeros((n,) + src.shape[1:], dtype=src.dtype)
    return out.index_add_(0, seg, src)

def _pad(x, pw):
    flat = tuple((v for pair in reversed(pw) for v in pair))
    return torch.nn.functional.pad(x, flat)
kernel_name = 'parallel_sum__memory_aug_recurrent_mem_tr__moe_arch_mod__prompt'

def setup_inputs():

    _w = lambda a: (lambda t: t.to(torch.bfloat16) if t.is_floating_point() else t)(torch.as_tensor(a))

    def _A_setup():
        B, S, Dm = (1, 2048, 512)
        H, Dh = (32, 128)
        M, seg = (8, 32)
        seg = min(seg, S)
        S = S // seg * seg
        return {'x': _w((lambda *s: torch.randn(*s, dtype=torch.bfloat16) * 0.05)(B, S, Dm)), 'mem0': _w((lambda *s: torch.randn(*s, dtype=torch.bfloat16) * 1.6)(B, M, Dm)), 'Wq': _w((lambda *s: torch.randn(*s, dtype=torch.bfloat16) * 0.8)(Dm, H, Dh) * Dm ** (-0.5)), 'Wk': _w((lambda *s: torch.randn(*s, dtype=torch.bfloat16) * 0.8)(Dm, H, Dh) * Dm ** (-0.5)), 'Wv': _w((lambda *s: torch.randn(*s, dtype=torch.bfloat16) * 1.6)(Dm, H, Dh) * Dm ** (-0.5)), 'Wo': _w((lambda *s: torch.randn(*s, dtype=torch.bfloat16) * 1.6)(H, Dh, Dm) * (H * Dh) ** (-0.5)), 'seg_len': seg}
    _A_in = _A_setup()

    def _B_setup():
        B, S, D, F, C = (1, 2048, 512, 1792, 8)
        return {'x': _w((lambda *s: torch.randn(*s, dtype=torch.bfloat16) * 0.05)(B, S, D)), 'w_r': _w((lambda *s: torch.randn(*s, dtype=torch.bfloat16) * 0.05)(D)), 'w_gate': _w((lambda *s: torch.randn(*s, dtype=torch.bfloat16) * 0.05)(D, F)), 'w_up': _w((lambda *s: torch.randn(*s, dtype=torch.bfloat16) * 0.05)(D, F)), 'w_down': _w((lambda *s: torch.randn(*s, dtype=torch.bfloat16) * 0.05)(F, D))}
    _B_in = _B_setup()
    return {'A_x': _A_in['x'], 'A_mem0': _A_in['mem0'], 'A_Wq': _A_in['Wq'], 'A_Wk': _A_in['Wk'], 'A_Wv': _A_in['Wv'], 'A_Wo': _A_in['Wo'], 'A_seg_len': _A_in['seg_len'], 'B_x': _B_in['x'], 'B_w_r': _B_in['w_r'], 'B_w_gate': _B_in['w_gate'], 'B_w_up': _B_in['w_up'], 'B_w_down': _B_in['w_down']}

def reference(A_x, A_mem0, A_Wq, A_Wk, A_Wv, A_Wo, A_seg_len, B_x, B_w_r, B_w_gate, B_w_up, B_w_down):

    def _A_ref(x, mem0, Wq, Wk, Wv, Wo, seg_len):
        B, S, Dm = x.shape
        H, Dh = (Wq.shape[1], Wq.shape[2])
        M = mem0.shape[1]
        scale = Dh ** (-0.5)
        n_seg = S // seg_len
        L = M + seg_len
        idx = torch.arange(L)
        mask = (idx[:, None] < M) | (idx[:, None] >= idx[None, :])
        mem = mem0
        outs = []
        for s in range(n_seg):
            xs = x[:, s * seg_len:(s + 1) * seg_len, :].clone()
            h = torch.concatenate([mem, xs], axis=1)
            q = torch.einsum('btd,dhe->bhte', h, Wq)
            k = torch.einsum('btd,dhe->bhte', h, Wk)
            v = torch.einsum('btd,dhe->bhte', h, Wv)
            scores = torch.einsum('bhqe,bhke->bhqk', q, k) * scale
            scores = torch.where(mask[None, None], scores, -1000000000.0)
            p = _softmax(scores, axis=-1)
            o = torch.einsum('bhqk,bhke->bhqe', p, v)
            o = torch.einsum('bhte,hed->btd', o, Wo)
            mem = o[:, :M, :].clone()
            outs.append(o[:, M:, :])
        return torch.concatenate(outs, axis=1)

    def _B_ref(x, w_r, w_gate, w_up, w_down):
        B, S, D = x.shape
        C = 8
        r = x @ w_r
        idx = torch.argsort(-r, axis=-1)[:, :C]
        idx_e = (lambda x, n, axis: torch.repeat_interleave(x, n, dim=axis))(idx.reshape(B, C, 1), D, axis=2)
        x_sel = (lambda x, i, axis: torch.take_along_dim(x, i, dim=axis))(x, idx_e, axis=1)
        g = _silu(x_sel @ w_gate)
        u = x_sel @ w_up
        y = g * u @ w_down
        r_sel = _sigmoid((lambda x, i, axis: torch.take_along_dim(x, i, dim=axis))(r, idx, axis=1))
        y = y * r_sel.reshape(B, C, 1)
        b_off = (torch.arange(B) * S).reshape(B, 1)
        flat_idx = (idx + b_off).reshape(B * C)
        zero = (x * 0.0).reshape(B * S, D)
        upd = _scatter_add(zero, flat_idx, y.reshape(B * C, D), axis=0)
        return x + upd.reshape(B, S, D)
    x = A_x
    xn = _layernorm(x, axis=-1)
    return x + _A_ref(xn, A_mem0, A_Wq, A_Wk, A_Wv, A_Wo, A_seg_len) + _B_ref(xn, B_w_r, B_w_gate, B_w_up, B_w_down)
```

```cpp
#include <hip/hip_runtime.h>
#include <hip/hip_bf16.h>
#include <stdint.h>

#define GAS __attribute__((address_space(1)))
#define LAS __attribute__((address_space(3)))
typedef unsigned short u16;
typedef __bf16 bf16x8 __attribute__((ext_vector_type(8)));
typedef __bf16 bf16x16 __attribute__((ext_vector_type(16)));
typedef __bf16 bf16x2 __attribute__((ext_vector_type(2)));
typedef float f32x4 __attribute__((ext_vector_type(4)));
typedef float f32x2 __attribute__((ext_vector_type(2)));
typedef float f32x16 __attribute__((ext_vector_type(16)));
typedef unsigned u32x4 __attribute__((ext_vector_type(4)));
typedef unsigned u32x2 __attribute__((ext_vector_type(2)));
typedef unsigned u32x8 __attribute__((ext_vector_type(8)));
typedef GAS unsigned gu32;
#define RLX_AGENT __ATOMIC_RELAXED, __HIP_MEMORY_SCOPE_AGENT

constexpr int S_ = 2048, DM = 512, NH = 32, DH = 128, HD = 4096, NSEG = 64, FF = 1792, TOPC = 8;
constexpr int NCOL = 64;

constexpr size_t CTRL_BYTES = 131072;
constexpr int RM = 8, RP = 6;
constexpr unsigned MREP = 4 * 8192u, PBASE = RM * MREP, PSLOT = 3 * 8192u, PREP = 4 * PSLOT, TAGU = PBASE + RP * PREP;
constexpr size_t EX_BYTES = (size_t)(TAGU + 8192u) * 2;
constexpr size_t OFF_XN = 4u << 20;
constexpr size_t OFF_KX = OFF_XN + (size_t)S_ * DM * 2;
constexpr size_t OFF_QX = OFF_KX + (size_t)S_ * HD * 2;
constexpr size_t OFF_VX = OFF_QX + (size_t)S_ * HD * 2;
constexpr size_t OFF_U = OFF_VX + (size_t)S_ * HD * 2;
constexpr size_t U_SC = 160 * 128;
constexpr size_t OFF_WVO = OFF_U + (size_t)NSEG * NCOL * U_SC;
constexpr size_t OFF_PX = OFF_WVO + (size_t)NCOL * 16 * 8 * 64 * 32;
constexpr size_t OFF_STAT = OFF_PX + (size_t)NSEG * NH * 5 * 512;
constexpr size_t OFF_GU = OFF_STAT + (size_t)S_ * 16;
constexpr size_t OFF_Y = OFF_GU + TOPC * FF * 4;
constexpr size_t OFF_DBG = OFF_Y + TOPC * DM * 4;
constexpr size_t WS_END = OFF_DBG + (1u << 20);

constexpr int C_RC = 0, C_OVF = 256, C_TOT = 288, C_POL = 320, C_TMO = 352, C_GU = 384, C_Y = 416;
constexpr int C_XN = 512, C_WVO = 768, C_RDY = 1024;
constexpr int C_V = 2048;
constexpr int C_KQ = 4096, C_U = 6144;
constexpr int C_FP = 8192, C_FUM = 9216;

#define D_H 3600
#define D_M 800
#define D_P 1700

#define TO_SLEEP 8
struct Params {
  const u16 *x, *mem0, *Wq, *Wk, *Wv, *Wo, *w_r, *w_gate, *w_up, *w_down;
  u16* out;
  unsigned char* ws;
  int dh, dm, dp, pad;
};

__device__ __forceinline__ unsigned f2u(float f) { return __builtin_bit_cast(unsigned, f); }
__device__ __forceinline__ float u2f(unsigned u) { return __builtin_bit_cast(float, u); }
__device__ __forceinline__ float bf2f(u16 b) { return u2f((unsigned)b << 16); }
__device__ __forceinline__ float bflo(unsigned u) { return u2f(u << 16); }
__device__ __forceinline__ float bfhi(unsigned u) { return u2f(u & 0xffff0000u); }
__device__ __forceinline__ u16 f2bf(float f) { __bf16 b = (__bf16)f; return __builtin_bit_cast(u16, b); }
__device__ __forceinline__ float bfr(float f) { return bf2f(f2bf(f)); }
__device__ __forceinline__ unsigned pk2(float lo, float hi) { f32x2 v = {lo, hi}; bf16x2 b = __builtin_convertvector(v, bf16x2); return __builtin_bit_cast(unsigned, b); }

constexpr int AUX_SC1 = 16;
constexpr unsigned SENT = 0xFFFFFFFFu;
constexpr unsigned SPIN_CAP = 1u << 15;
constexpr unsigned WAIT_CAP = 1u << 17;
__device__ __forceinline__ unsigned xcc_id() { return (unsigned)__builtin_amdgcn_s_getreg(20 | (3 << 11)) & 0xfu; }
__device__ __forceinline__ unsigned now32() { unsigned long long t; asm volatile("s_memtime %0\n\ts_waitcnt lgkmcnt(0)" : "=s"(t)::"memory"); return (unsigned)t; }
__device__ __forceinline__ u32x4 ld_sc1(__amdgpu_buffer_rsrc_t rs, unsigned off) { return __builtin_amdgcn_raw_buffer_load_b128(rs, off, 0, AUX_SC1); }
__device__ __forceinline__ void st_sc1(__amdgpu_buffer_rsrc_t rs, unsigned off, u32x4 v) { __builtin_amdgcn_raw_buffer_store_b128(v, rs, off, 0, AUX_SC1); }
__device__ __forceinline__ void st_sc1s(__amdgpu_buffer_rsrc_t rs, unsigned off, unsigned soff, u32x4 v) { __builtin_amdgcn_raw_buffer_store_b128(v, rs, off, soff, AUX_SC1); }
__device__ __forceinline__ void st_sc1_64(__amdgpu_buffer_rsrc_t rs, unsigned off, u32x2 v) { __builtin_amdgcn_raw_buffer_store_b64(v, rs, off, 0, AUX_SC1); }
__device__ __forceinline__ void st_sc1_32(__amdgpu_buffer_rsrc_t rs, unsigned off, unsigned v) { __builtin_amdgcn_raw_buffer_store_b32(v, rs, off, 0, AUX_SC1); }
__device__ __forceinline__ unsigned nosent(unsigned x) { return min(x, 0xFFFFFFFEu); }
__device__ __forceinline__ f32x4 mfma16(u32x4 a, u32x4 b, f32x4 c) { return __builtin_amdgcn_mfma_f32_16x16x32_bf16(__builtin_bit_cast(bf16x8, a), __builtin_bit_cast(bf16x8, b), c, 0, 0, 0); }
__device__ __forceinline__ f32x16 mfma32(u32x4 a, u32x4 b, f32x16 c) { return __builtin_amdgcn_mfma_f32_32x32x16_bf16(__builtin_bit_cast(bf16x8, a), __builtin_bit_cast(bf16x8, b), c, 0, 0, 0); }
__device__ __forceinline__ f32x4 smfma(u32x4 a, u32x8 b, f32x4 c, int idx) { return __builtin_amdgcn_smfmac_f32_16x16x64_bf16(__builtin_bit_cast(bf16x8, a), __builtin_bit_cast(bf16x16, b), c, idx, 0, 0); }
typedef short v4i16_t __attribute__((ext_vector_type(4)));
__device__ __forceinline__ u32x2 tr_read(LAS unsigned char* p) { return __builtin_bit_cast(u32x2, __builtin_amdgcn_ds_read_tr16_b64_v4i16((LAS v4i16_t*)p)); }
__device__ __forceinline__ unsigned ex_addr(unsigned i, unsigned pol) { return (i >> 13) * 16384u + (i & 8191u) + pol * 8192u; }
__device__ __forceinline__ unsigned dpp_ror8(unsigned x) { return (unsigned)__builtin_amdgcn_update_dpp(0, (int)x, 0x128, 0xf, 0xf, true); }
#define BAR() do { asm volatile("" ::: "memory"); __builtin_amdgcn_s_waitcnt(0xC07F); __builtin_amdgcn_s_barrier(); asm volatile("" ::: "memory"); } while (0)
#define DRAIN() asm volatile("s_waitcnt vmcnt(0)" ::: "memory")
#define GLDS(src, dst) __builtin_amdgcn_global_load_lds((const GAS unsigned*)(src), (LAS unsigned*)(dst), 16, 0, 0)
#define DM_NOPS "s_nop 15\n\ts_nop 15"
#define DM_NOPS2 "s_nop 7"
#define DP_NOPS "s_nop 15\n\ts_nop 15\n\ts_nop 15\n\ts_nop 7"
#define DP_NOPS2 "s_nop 15"

#define TRG(id, k) do {} while (0)
#define TRT(k) do {} while (0)
#define TRU(k) do {} while (0)

struct Ctx {
  __amdgpu_buffer_rsrc_t ex;
  u32x4 exs;
  __amdgpu_buffer_rsrc_t ws;
  gu32* ctrl;
  unsigned pol;
  int wave, lane;
};

__device__ __forceinline__ int lane_id() { int l; asm volatile("v_mbcnt_lo_u32_b32 %0, -1, 0\n\tv_mbcnt_hi_u32_b32 %0, -1, %0" : "=v"(l)); return l; }
__device__ __forceinline__ Ctx fresh(const Ctx& c) { Ctx r = c; r.lane = lane_id(); return r; }
__device__ __forceinline__ void arrive(const Ctx& cx, int word) { __hip_atomic_fetch_add(cx.ctrl + word, 1u, RLX_AGENT); }
__device__ __forceinline__ void wait_words(const Ctx& cx, int word, int n, unsigned want) {
  gu32* a = cx.ctrl + word + (cx.lane < n ? cx.lane : 0) * 32;
  for (unsigned spins = 0;; ++spins) {
    const unsigned v = __hip_atomic_load(a, RLX_AGENT) + 1u;
    if (__all(v >= want)) break;
    if (spins > WAIT_CAP) { __hip_atomic_store(cx.ctrl + C_TMO, 1u, RLX_AGENT); break; }
    if ((spins & 255u) == 255u && __builtin_amdgcn_readfirstlane(__hip_atomic_load(cx.ctrl + C_TMO, RLX_AGENT)) == 1u) break;
    __builtin_amdgcn_s_sleep(2);
  }
  __builtin_amdgcn_fence(__ATOMIC_ACQUIRE, "wavefront");
}
__device__ __forceinline__ void timed_wait(unsigned tref, int d) {
  while ((int)(now32() - tref) < d - 200) __builtin_amdgcn_s_sleep(1);
  while ((int)(now32() - tref) < d) {}
}

__device__ __forceinline__ u32x2 front(const Params& p, const Ctx& cx, LAS unsigned char* MEMIMG, const u32x8 (&W)[2][8], int s, unsigned& tref) {
  const int w = cx.wave, l = cx.lane;
  u32x4 v, v1 = {0, 0, 0, 0}, v2 = v1, v3 = v1, v0 = v1;
  if (s == 0) {
    const u16* mp = p.mem0 + (l & 7) * DM + (8 * w + (l >> 3)) * 8;
    asm volatile("global_load_dwordx4 %0, %1, off\n\ts_waitcnt vmcnt(0)" : "=&{v[236:239]}"(v) : "v"(mp) : "memory");
  } else {
    const unsigned off = ex_addr((unsigned)(s & 3) * 8192u + w * 1024 + l * 16, cx.pol);
    unsigned t_, cnt, sr;
    asm volatile(
      "s_mov_b32 %[sr], 0\n\t"
      "buffer_load_dwordx4 v[240:243], %[off], %[rs], %[sr] offen sc1\n\t"
      "s_add_u32 %[sr], %[sr], 65536\n\t"
      "s_cmp_eq_u32 %[sr], 524288\n\t"
      "s_cselect_b32 %[sr], 0, %[sr]\n\t"
      DM_NOPS "\n\t"
      "buffer_load_dwordx4 v[244:247], %[off], %[rs], %[sr] offen sc1\n\t"
      "s_add_u32 %[sr], %[sr], 65536\n\t"
      "s_cmp_eq_u32 %[sr], 524288\n\t"
      "s_cselect_b32 %[sr], 0, %[sr]\n\t"
      DM_NOPS "\n\t"
      "buffer_load_dwordx4 v[248:251], %[off], %[rs], %[sr] offen sc1\n\t"
      "s_add_u32 %[sr], %[sr], 65536\n\t"
      "s_cmp_eq_u32 %[sr], 524288\n\t"
      "s_cselect_b32 %[sr], 0, %[sr]\n\t"
      DM_NOPS "\n\t"
      "buffer_load_dwordx4 v[252:255], %[off], %[rs], %[sr] offen sc1\n\t"
      "s_add_u32 %[sr], %[sr], 65536\n\t"
      "s_cmp_eq_u32 %[sr], 524288\n\t"
      "s_cselect_b32 %[sr], 0, %[sr]\n\t"
      "s_mov_b32 %[cnt], 0\n\t"
      "L%=_1:\n\t"
      "s_waitcnt vmcnt(3)\n\t"
      "v_max3_u32 %[t], v240, v241, v242\n\t"
      "v_max_u32 %[t], %[t], v243\n\t"
      "v_cmp_eq_u32 vcc, -1, %[t]\n\t"
      "s_cbranch_vccz L%=_10\n\t"
      "buffer_load_dwordx4 v[240:243], %[off], %[rs], %[sr] offen sc1\n\t"
      "s_add_u32 %[sr], %[sr], 65536\n\t"
      "s_cmp_eq_u32 %[sr], 524288\n\t"
      "s_cselect_b32 %[sr], 0, %[sr]\n\t"
      "s_add_u32 %[cnt], %[cnt], 1\n\t"
      DM_NOPS2 "\n\t"
      "s_waitcnt vmcnt(3)\n\t"
      "v_max3_u32 %[t], v244, v245, v246\n\t"
      "v_max_u32 %[t], %[t], v247\n\t"
      "v_cmp_eq_u32 vcc, -1, %[t]\n\t"
      "s_cbranch_vccz L%=_11\n\t"
      "buffer_load_dwordx4 v[244:247], %[off], %[rs], %[sr] offen sc1\n\t"
      "s_add_u32 %[sr], %[sr], 65536\n\t"
      "s_cmp_eq_u32 %[sr], 524288\n\t"
      "s_cselect_b32 %[sr], 0, %[sr]\n\t"
      "s_add_u32 %[cnt], %[cnt], 1\n\t"
      DM_NOPS2 "\n\t"
      "s_waitcnt vmcnt(3)\n\t"
      "v_max3_u32 %[t], v248, v249, v250\n\t"
      "v_max_u32 %[t], %[t], v251\n\t"
      "v_cmp_eq_u32 vcc, -1, %[t]\n\t"
      "s_cbranch_vccz L%=_12\n\t"
      "buffer_load_dwordx4 v[248:251], %[off], %[rs], %[sr] offen sc1\n\t"
      "s_add_u32 %[sr], %[sr], 65536\n\t"
      "s_cmp_eq_u32 %[sr], 524288\n\t"
      "s_cselect_b32 %[sr], 0, %[sr]\n\t"
      "s_add_u32 %[cnt], %[cnt], 1\n\t"
      DM_NOPS2 "\n\t"
      "s_waitcnt vmcnt(3)\n\t"
      "v_max3_u32 %[t], v252, v253, v254\n\t"
      "v_max_u32 %[t], %[t], v255\n\t"
      "v_cmp_eq_u32 vcc, -1, %[t]\n\t"
      "s_cbranch_vccz L%=_13\n\t"
      "buffer_load_dwordx4 v[252:255], %[off], %[rs], %[sr] offen sc1\n\t"
      "s_add_u32 %[sr], %[sr], 65536\n\t"
      "s_cmp_eq_u32 %[sr], 524288\n\t"
      "s_cselect_b32 %[sr], 0, %[sr]\n\t"
      "s_add_u32 %[cnt], %[cnt], 1\n\t"
      DM_NOPS2 "\n\t"
      "s_cmp_lt_u32 %[cnt], %[cap]\n\t"
      "s_cbranch_scc1 L%=_1\n\t"
      "s_waitcnt vmcnt(0)\n\t"
      "L%=_10:\n\t"
      "v_mov_b32 v236, v240\n\t"
      "v_mov_b32 v237, v241\n\t"
      "v_mov_b32 v238, v242\n\t"
      "v_mov_b32 v239, v243\n\t"
      "s_branch L%=_20\n\t"
      "L%=_11:\n\t"
      "v_mov_b32 v236, v244\n\t"
      "v_mov_b32 v237, v245\n\t"
      "v_mov_b32 v238, v246\n\t"
      "v_mov_b32 v239, v247\n\t"
      "s_branch L%=_20\n\t"
      "L%=_12:\n\t"
      "v_mov_b32 v236, v248\n\t"
      "v_mov_b32 v237, v249\n\t"
      "v_mov_b32 v238, v250\n\t"
      "v_mov_b32 v239, v251\n\t"
      "s_branch L%=_20\n\t"
      "L%=_13:\n\t"
      "v_mov_b32 v236, v252\n\t"
      "v_mov_b32 v237, v253\n\t"
      "v_mov_b32 v238, v254\n\t"
      "v_mov_b32 v239, v255\n\t"
      "L%=_20:\n\t"
      : "=&{v[240:243]}"(v0), "=&{v[244:247]}"(v1), "=&{v[248:251]}"(v2), "=&{v[252:255]}"(v3), "=&{v[236:239]}"(v), [t] "=&v"(t_), [cnt] "=&s"(cnt), [sr] "=&s"(sr)
      : [off] "v"(off), [rs] "s"(cx.exs), [cap] "s"(SPIN_CAP)
      : "vcc", "scc", "memory");
    if (cnt >= SPIN_CAP) __hip_atomic_store(cx.ctrl + C_TMO, 1u, RLX_AGENT);
  }
  LAS unsigned char* mb = MEMIMG + l * 16;
  *(LAS u32x4*)(mb + w * 1024) = v;
  const int idx = (l & 8) ? 0xEEEE : 0x4444;
  f32x4 a0 = {0.f, 0.f, 0.f, 0.f}, a1 = {0.f, 0.f, 0.f, 0.f};
  a0 = smfma(v, W[0][0], a0, idx);
  a1 = smfma(v, W[1][0], a1, idx);
  asm volatile("" : "+v"(a0), "+v"(a1));
  BAR();
  u32x4 a[7];
#pragma unroll
  for (int i = 1; i < 8; ++i) a[i - 1] = *(LAS u32x4*)(mb + ((w + i) & 7) * 1024);
  __builtin_amdgcn_sched_barrier(0);
#pragma unroll
  for (int i = 1; i < 8; ++i) {
    a0 = smfma(a[i - 1], W[0][i], a0, idx);
    a1 = smfma(a[i - 1], W[1][i], a1, idx);
    if (i == 5) {
      asm volatile("" : "+v"(a0), "+v"(a1));
      tref = now32();
      asm volatile("s_waitcnt vmcnt(0)" : "+{v[240:243]}"(v0), "+{v[244:247]}"(v1), "+{v[248:251]}"(v2), "+{v[252:255]}"(v3)::"memory");
      __builtin_amdgcn_sched_barrier(0);
    }
  }
  float o[4];
#pragma unroll
  for (int r = 0; r < 4; ++r) {
    auto sw = __builtin_amdgcn_permlane32_swap(f2u(a0[r]), f2u(a1[r]), false, false);
    o[r] = u2f(sw[0]) + u2f(sw[1]);
  }
  u32x2 res; res.x = pk2(o[0], o[1]); res.y = pk2(o[2], o[3]);
  return res;
}

constexpr float SCALE = 0.08838834764831845f;
constexpr float LOG2E = 1.4426950408889634f;
__device__ __forceinline__ float vmax(float a, float b) { float r; asm("v_max_f32 %0, %1, %2" : "=v"(r) : "v"(a), "v"(b)); return r; }
__device__ __forceinline__ float vmax3(float a, float b, float c) { float r; asm("v_max3_f32 %0, %1, %2, %3" : "=v"(r) : "v"(a), "v"(b), "v"(c)); return r; }
template <bool MAXOP> __device__ __forceinline__ float red_g(float v) {
  {
    auto r = __builtin_amdgcn_permlane16_swap(f2u(v), f2u(v), false, false);
    const float a = u2f(r[0]), b = u2f(r[1]);
    v = MAXOP ? vmax(a, b) : a + b;
  }
  {
    auto r = __builtin_amdgcn_permlane32_swap(f2u(v), f2u(v), false, false);
    const float a = u2f(r[0]), b = u2f(r[1]);
    v = MAXOP ? vmax(a, b) : a + b;
  }
  return v;
}

template <int ROLE>
__device__ __forceinline__ void head_loop(const Params& p, const Ctx& cx_, LAS unsigned char* L, int h, const u32x8 (&W)[2][8]) {
  const Ctx cx = fresh(cx_);
  const int w = cx.wave, l = cx.lane, g = l >> 4, n = l & 15;
  LAS unsigned char* MEMIMG = L;
  LAS unsigned char* QKIMG = L + 8192;
  LAS unsigned* XCNT = (LAS unsigned*)(L + 12288);
  LAS unsigned char* KQ = L + 16384;
  auto stage = [&](int s) {
    wait_words(cx, C_KQ + s * 32, 1, 32u);
    const unsigned kb = (unsigned)((w == 4 ? OFF_KX : OFF_QX) + ((size_t)s * NH + h) * 8192) + l * 16;
    LAS unsigned char* d = KQ + (s & 1) * 16384 + (w == 4 ? 0 : 8192) + l * 16;
    u32x4 pf[8];
#pragma unroll
    for (int i = 0; i < 8; ++i) pf[i] = ld_sc1(cx.ws, kb + i * 1024);
#pragma unroll
    for (int i = 0; i < 8; ++i) *(LAS u32x4*)(d + i * 1024) = pf[i];
  };
  if (ROLE == 2) stage(0);
  unsigned tref = 0;
  const int q4 = (l & 15) >> 2, pp = l & 3;
  LAS unsigned char* trb = QKIMG + (16 * (g >> 1) + 4 * (g & 1) + q4) * 32;
  const bool up = n >= 8;
  LAS unsigned char* qkw = QKIMG + (((2 * w + (l >> 5)) & 7) * 16 + n) * 32 + (w >> 2) * 16 + (g & 1) * 8;
  const unsigned pboff = PBASE + h * 640 + (n & 7) * 16 + (up ? 512 : g * 128);
  for (int s = 0; s < NSEG; ++s) {
    if (s > 0) timed_wait(tref, p.dh);
    __builtin_amdgcn_s_setprio(2);
    const u32x2 qk = front(p, cx, MEMIMG, W, s, tref);
    *(LAS u32x2*)qkw = qk;
    LAS unsigned char* KI = KQ + (s & 1) * 16384 + g * 512 + n * 16;
    if (ROLE == 0) {
      u32x4 Kf[2][4];
#pragma unroll
      for (int t = 0; t < 4; ++t) { Kf[0][t] = *(LAS u32x4*)(KI + t * 2048); Kf[1][t] = *(LAS u32x4*)(KI + t * 2048 + 256); }
      const unsigned paddr = ex_addr((unsigned)(s & 3) * PSLOT + pboff, cx.pol);
      BAR();
      __builtin_amdgcn_s_setprio(3);
      if (s == NSEG - 1) break;
      u32x4 F[4], F2[4];
#pragma unroll
      for (int t = 0; t < 4; ++t) {
        const u32x2 lo2 = tr_read(trb + t * 1024 + (pp & 1) * 8), hi2 = tr_read(trb + t * 1024 + 256 + (pp & 1) * 8);
        F2[t] = (u32x4){lo2.x, lo2.y, hi2.x, hi2.y};
      }
#pragma unroll
      for (int t = 0; t < 4; ++t) {
        const u32x2 lo = tr_read(trb + t * 1024 + pp * 8), hi = tr_read(trb + t * 1024 + 256 + pp * 8);
        F[t] = (u32x4){lo.x, lo.y, hi.x, hi.y};
      }
      __builtin_amdgcn_sched_barrier(0);
      f32x4 s0 = {0.f, 0.f, 0.f, 0.f}, s1 = s0, sm = s0;
#pragma unroll
      for (int t = 0; t < 4; ++t) { s0 = mfma16(Kf[0][t], F2[t], s0); s1 = mfma16(Kf[1][t], F2[t], s1); }
#pragma unroll
      for (int t = 0; t < 4; ++t) sm = mfma16(F[t], F2[t], sm);
      float v[6];
#pragma unroll
      for (int r = 0; r < 4; ++r) v[r] = up ? s1[r] : s0[r];
      float mx = vmax(vmax3(v[0], v[1], v[2]), v[3]);
      v[4] = (g >= 2) ? (up ? sm[2] : sm[0]) : -INFINITY;
      v[5] = (g >= 2) ? (up ? sm[3] : sm[1]) : -INFINITY;
      mx = vmax3(mx, v[4], v[5]);
      mx = vmax(mx, u2f(dpp_ror8(f2u(mx))));
      mx = red_g<true>(mx);
      const float cs = SCALE * LOG2E, mc = -mx * cs;
#pragma unroll
      for (int i = 0; i < 6; ++i) v[i] = __builtin_amdgcn_exp2f(__builtin_fmaf(v[i], cs, mc));
      float sum = ((v[0] + v[1]) + (v[2] + v[3])) + (v[4] + v[5]);
      sum += u2f(dpp_ror8(f2u(sum)));
      sum = red_g<false>(sum);
      const float inv = __builtin_amdgcn_rcpf(sum);
      const unsigned px0 = nosent(pk2(v[0] * inv, v[1] * inv)), px1 = nosent(pk2(v[2] * inv, v[3] * inv));
      const unsigned pm = nosent(pk2(v[4] * inv, v[5] * inv));
      const unsigned o0 = dpp_ror8(px0), o1 = dpp_ror8(px1), t1 = dpp_ror8(pm);
      const unsigned d2 = __builtin_amdgcn_permlane16_swap(t1, t1, false, false)[1], d3 = __builtin_amdgcn_permlane16_swap(pm, pm, false, false)[1];
      u32x4 dat; dat.x = up ? t1 : px0; dat.y = up ? pm : px1; dat.z = up ? d2 : o0; dat.w = up ? d3 : o1;
      if (!up || g == 2) {
#pragma unroll
        for (int r = 0; r < RP; ++r) st_sc1s(cx.ex, paddr, r * 2 * PREP, dat);
      }
      __builtin_amdgcn_s_setprio(0);
      if (l < 40) {
        const unsigned ra = ex_addr(PBASE + (unsigned)((s + 2) & 3) * PSLOT + h * 640 + l * 16, cx.pol);
#pragma unroll
        for (int r = 0; r < RP; ++r) st_sc1s(cx.ex, ra, r * 2 * PREP, (u32x4){SENT, SENT, SENT, SENT});
      }
    } else {
      BAR();
      __builtin_amdgcn_s_setprio(0);
      if (ROLE == 2) {
        __builtin_amdgcn_s_sleep(3);
        if (s + 1 < NSEG) stage(s + 1);
      } else if (ROLE == 3) {
        __builtin_amdgcn_s_sleep(3);
        const int qt = w - 5;
        u32x4 F[4], Kf[2][4], Qf[4];
#pragma unroll
        for (int t = 0; t < 4; ++t) {
          const u32x2 lo = tr_read(trb + t * 1024 + pp * 8), hi = tr_read(trb + t * 1024 + 256 + pp * 8);
          F[t] = (u32x4){lo.x, lo.y, hi.x, hi.y};
          Kf[0][t] = *(LAS u32x4*)(KI + t * 2048); Kf[1][t] = *(LAS u32x4*)(KI + t * 2048 + 256);
          Qf[t] = *(LAS u32x4*)(KI + 8192 + t * 2048 + qt * 256);
        }
        f32x4 s0 = {0.f, 0.f, 0.f, 0.f}, s1 = s0, sm = s0;
#pragma unroll
        for (int t = 0; t < 4; ++t) { s0 = mfma16(Kf[0][t], Qf[t], s0); s1 = mfma16(Kf[1][t], Qf[t], s1); sm = mfma16(F[t], Qf[t], sm); }
        const int qi = qt * 16 + n;
        float sc[12];
#pragma unroll
        for (int r = 0; r < 4; ++r) {
          sc[r] = (4 * g + r <= qi) ? s0[r] : -INFINITY;
          sc[4 + r] = (16 + 4 * g + r <= qi) ? s1[r] : -INFINITY;
          sc[8 + r] = (g >= 2) ? sm[r] : -INFINITY;
        }
        float mx = vmax3(vmax3(sc[0], sc[1], sc[2]), vmax3(sc[3], sc[4], sc[5]), vmax3(vmax3(sc[6], sc[7], sc[8]), vmax3(sc[9], sc[10], sc[11]), sc[0]));
        mx = red_g<true>(mx);
        const float cs = SCALE * LOG2E, mc = -mx * cs;
        float sum = 0.f;
#pragma unroll
        for (int i = 0; i < 12; ++i) { sc[i] = __builtin_amdgcn_exp2f(__builtin_fmaf(sc[i], cs, mc)); sum += sc[i]; }
        sum = red_g<false>(sum);
        const float inv = __builtin_amdgcn_rcpf(sum);
        u32x4 px; px.x = pk2(sc[0] * inv, sc[1] * inv); px.y = pk2(sc[2] * inv, sc[3] * inv); px.z = pk2(sc[4] * inv, sc[5] * inv); px.w = pk2(sc[6] * inv, sc[7] * inv);
        u32x2 pm; pm.x = pk2(sc[8] * inv, sc[9] * inv); pm.y = pk2(sc[10] * inv, sc[11] * inv);
        const unsigned pb = (unsigned)(OFF_PX + ((size_t)s * NH + h) * 2560) + qi * 16;
        st_sc1(cx.ws, pb + g * 512, px);
        if (g >= 2) st_sc1_64(cx.ws, pb + 4 * 512 + (g - 2) * 8, pm);
        DRAIN();
        if (l == 0) {
          const unsigned old = __hip_atomic_fetch_add(XCNT, 1u, __ATOMIC_RELAXED, __HIP_MEMORY_SCOPE_WORKGROUP);
          if (old & 1u) __hip_atomic_store(cx.ctrl + C_FP + h * 32, (unsigned)s, RLX_AGENT);
        }
      }
    }
  }
}

__device__ __forceinline__ void chain_ready(const Ctx& cx_, int id) {
  const Ctx cx = fresh(cx_);
  DRAIN();
  BAR();
  if (cx.wave == 0) {
    if (cx.lane == 0) arrive(cx, C_RDY + (id & 7) * 32);
    wait_words(cx, C_RDY, 8, 12u);
  }
  BAR();
}

__device__ __forceinline__ void head_role(const Params& p, const Ctx& cx_, LAS unsigned char* L, int h) {
  const Ctx cx = fresh(cx_);
  const int w = cx.wave, l = cx.lane, g = l >> 4, tid = w * 64 + l;
  u32x8 W[2][8];
#pragma unroll
  for (int rd = 0; rd < 2; ++rd) {
    const u16* Wsrc = rd ? p.Wk : p.Wq;
#pragma unroll
    for (int i = 0; i < 16; ++i) {
      const int kap = i * 4 + (l >> 4), sig = l & 15;
      const int gsw = (kap & 3) | (((kap >> 3) & 1) << 2);
      const int chunk = (((sig >> 1) ^ gsw) << 1) | (sig & 1);
      const int d = 64 * w + 16 * (kap >> 4) + 8 * ((kap >> 1) & 1) + 2 * ((kap >> 2) & 3) + (kap & 1);
      GLDS(Wsrc + (size_t)d * HD + h * DH + chunk * 8, L + w * 16384 + i * 1024);
    }
    if (rd == 0) {
#pragma unroll
      for (int k = 0; k < 2; ++k) {
        const int q = tid + k * 512;
        if (q < 4 * RP * 40) {
          const int pc = q % 40, sr = q / 40;
          st_sc1(cx.ex, ex_addr(PBASE + (unsigned)(sr >> 2) * PREP + (unsigned)(sr & 3) * PSLOT + h * 640 + pc * 16, cx.pol), (u32x4){SENT, SENT, SENT, SENT});
        }
      }
    }
    DRAIN();
    BAR();
    if ((w >> 2) == rd) {
      const int q = (l & 15) >> 2, pp = l & 3;
#pragma unroll
      for (int i = 0; i < 8; ++i) {
        LAS unsigned char* img = L + ((w + i) & 7) * 16384;
#pragma unroll
        for (int t = 0; t < 2; ++t) {
          const int et = (2 * w + t) & 7;
          u32x8 f;
#pragma unroll
          for (int jq = 0; jq < 4; ++jq) {
            const int kap = (jq >> 1) * 32 + 8 * g + (jq & 1) * 4 + q;
            const int gsw = (kap & 3) | (((kap >> 3) & 1) << 2);
            const u32x2 v = tr_read(img + kap * 256 + ((et ^ gsw) * 32) + pp * 8);
            f[jq * 2] = v.x; f[jq * 2 + 1] = v.y;
          }
          W[t][i] = f;
        }
      }
    }
    BAR();
  }
  if (tid == 0) *(LAS unsigned*)(L + 12288) = 0u;
  TRG(h, 1);
  chain_ready(cx, h);
  TRG(h, 2);
  if (w == 0) head_loop<0>(p, cx, L, h, W);
  else if (w < 4) head_loop<1>(p, cx, L, h, W);
  else if (w == 4 || w == 7) head_loop<2>(p, cx, L, h, W);
  else head_loop<3>(p, cx, L, h, W);
}

constexpr int WO_STRIDE = 1088;
__device__ __forceinline__ void wo_issue(const Params& p, const Ctx& cx_, LAS unsigned char* L, int pid, u32x4 (&A)[4][8]) {
  const Ctx cx = fresh(cx_);
  const int w = cx.wave, l = cx.lane, h = pid & 31, qr = pid >> 5;
#pragma unroll
  for (int i = 0; i < 16; ++i) {
    const int e = w * 16 + i;
    GLDS(p.Wo + ((size_t)h * DH + e) * DM + l * 8, L + e * WO_STRIDE);
  }
#pragma unroll
  for (int i = 0; i < 4; ++i) {
    const u16* src = p.Wv + (size_t)(32 * (qr * 4 + i) + (l & 31)) * HD + h * DH + 4 * (l >> 5);
#pragma unroll
    for (int u = 0; u < 8; ++u) {
      const u32x2 lo = *(const GAS u32x2*)(src + 16 * u), h2 = *(const GAS u32x2*)(src + 16 * u + 8);
      A[i][u] = (u32x4){lo.x, lo.y, h2.x, h2.y};
    }
  }
}
__device__ __forceinline__ void wvo_part(const Params& p, const Ctx& cx_, LAS unsigned char* L, int pid, const u32x4 (&A)[4][8], u32x4 (&B)[2][8]) {
  const Ctx cx = fresh(cx_);
  const int w = cx.wave, l = cx.lane, hi = l >> 5, h = pid & 31, qr = pid >> 5;
  {
    const int blk = (l >> 4) & 1, q = (l & 15) >> 2, pp = l & 3;
    LAS unsigned char* b0 = L + (4 * hi + q) * WO_STRIDE + (64 * w + 16 * blk + 4 * pp) * 2;
#pragma unroll
    for (int nt = 0; nt < 2; ++nt)
#pragma unroll
      for (int u = 0; u < 8; ++u) {
        const u32x2 lo = tr_read(b0 + (16 * u) * WO_STRIDE + nt * 64), hi2 = tr_read(b0 + (16 * u + 8) * WO_STRIDE + nt * 64);
        B[nt][u] = (u32x4){lo.x, lo.y, hi2.x, hi2.y};
      }
  }
#pragma unroll
  for (int i = 0; i < 4; ++i) {
    const int dt = qr * 4 + i;
#pragma unroll
    for (int nt = 0; nt < 2; ++nt) {
      f32x16 acc = {0.f};
#pragma unroll
      for (int u = 0; u < 8; ++u) acc = mfma32(A[i][u], B[nt][u], acc);
      const int col = 64 * w + 32 * nt + (l & 31);
#pragma unroll
      for (int b16 = 0; b16 < 2; ++b16) {
        const int d16 = 2 * dt + b16, T = d16 >> 2, gA = d16 & 3;
        const int lane2 = (2 * (gA & 1) + hi) * 16 + (h & 1) * 8 + (col & 7);
        u32x4 o;
        o.x = pk2(acc[8 * b16 + 0], acc[8 * b16 + 1]); o.y = pk2(acc[8 * b16 + 4], acc[8 * b16 + 5]);
        o.z = pk2(acc[8 * b16 + 2], acc[8 * b16 + 3]); o.w = pk2(acc[8 * b16 + 6], acc[8 * b16 + 7]);
        st_sc1(cx.ws, (unsigned)(OFF_WVO + ((((size_t)(col >> 3) * 16 + (h >> 1)) * 8 + T) * 2048) + (gA >> 1) * 1024 + lane2 * 16), o);
      }
      __builtin_amdgcn_sched_barrier(0);
    }
  }
  DRAIN();
  BAR();
  if (w == 0 && l == 0) arrive(cx, C_WVO + (pid & 7) * 32);
}

template <int ROLE>
__device__ __forceinline__ void col_loop(const Params& p, const Ctx& cx_, LAS unsigned char* L, int c, const u32x8 (&W)[2][8]) {
  const Ctx cx = fresh(cx_);
  const int w = cx.wave, l = cx.lane, g = l >> 4, n = l & 15, tid = w * 64 + l;
  LAS unsigned char* MEMIMG = L;
  LAS unsigned* UCNT = (LAS unsigned*)(L + 8192);
  LAS unsigned char* RED = L + 9216;
  LAS unsigned char* UIMG = L + 12288;
  const int ht = tid - 256;
  auto ustage = [&](int s) {
    wait_words(cx, C_U + s * 32, 1, 32u);
    const unsigned base = (unsigned)(OFF_U + ((size_t)s * NCOL + c) * U_SC);
    LAS unsigned char* UI = UIMG + (s & 1) * 20480;
    u32x4 ur[4]; unsigned uoff[4];
#pragma unroll
    for (int i = 0; i < 4; ++i) { const int ch4 = (ht >> 3) + 32 * i; uoff[i] = (unsigned)((((ch4 >> 2) * 5 + (ch4 & 3)) * 8 + (ht & 7)) * 16); ur[i] = ld_sc1(cx.ws, base + uoff[i]); }
#pragma unroll
    for (int i = 0; i < 4; ++i) *(LAS u32x4*)(UI + uoff[i]) = ur[i];
  };
  if (ROLE == 2) ustage(0);
  unsigned tb1 = 0, tpub = 0;
  const unsigned umo = (unsigned)((((2 * (2 * w + (l >> 5)) + (n >> 3)) * 5 + 4) * 8 + (n & 7)) * 16 + (g & 1) * 8);
  for (int s = 0; s < NSEG; ++s) {
    LAS unsigned char* UI = UIMG + (s & 1) * 20480;
    u32x4 A0[5], A1[5], A2[5];
    if (s > 0) timed_wait(tpub, p.dm);
    const u32x2 um = front(p, cx, MEMIMG, W, s, tb1);
    *(LAS u32x2*)(UI + umo) = um;
    BAR();
    if (ROLE == 2) {
      const unsigned o = (unsigned)((((ht >> 3) * 5 + 4) * 8 + (ht & 7)) * 16);
      const u32x4 v = *(LAS u32x4*)(UI + o);
      st_sc1(cx.ws, (unsigned)(OFF_U + ((size_t)s * NCOL + c) * U_SC) + o, v);
      if (s + 1 < NSEG - 1) ustage(s + 1);
      DRAIN();
      if (l == 0) {
        const unsigned old = __hip_atomic_fetch_add(UCNT, 1u, __ATOMIC_RELAXED, __HIP_MEMORY_SCOPE_WORKGROUP);
        if ((old & 3u) == 3u) __hip_atomic_store(cx.ctrl + C_FUM + c * 32, (unsigned)s, RLX_AGENT);
      }
      if (s == NSEG - 1) break;
    } else {
      if (s == NSEG - 1) break;
      u32x4 B[5];
      unsigned aoff[5];
#pragma unroll
      for (int i = 0; i < 5; ++i) {
        const int ch = (n >> 3) * 80 + 4 * (w * 5 + i) + g;
        B[i] = *(LAS u32x4*)(UI + (ch * 8 + (n & 7)) * 16);
        aoff[i] = ex_addr(PBASE + (unsigned)(s & 3) * PSLOT + ch * 128 + (n & 7) * 16, cx.pol);
      }
      timed_wait(tb1, p.dp);
      __builtin_amdgcn_s_setprio(3);
      f32x4 acc; unsigned t_, cnt, sr;
      asm volatile(
        "s_mov_b32 %[sr], 0\n\t"
        "buffer_load_dwordx4 v[196:199], %[o0], %[rs], %[sr] offen sc1\n\t"
        "buffer_load_dwordx4 v[200:203], %[o1], %[rs], %[sr] offen sc1\n\t"
        "buffer_load_dwordx4 v[204:207], %[o2], %[rs], %[sr] offen sc1\n\t"
        "buffer_load_dwordx4 v[208:211], %[o3], %[rs], %[sr] offen sc1\n\t"
        "buffer_load_dwordx4 v[212:215], %[o4], %[rs], %[sr] offen sc1\n\t"
        "s_add_u32 %[sr], %[sr], 196608\n\t"
        "s_cmp_eq_u32 %[sr], 1179648\n\t"
        "s_cselect_b32 %[sr], 0, %[sr]\n\t"
        DP_NOPS "\n\t"
        "buffer_load_dwordx4 v[216:219], %[o0], %[rs], %[sr] offen sc1\n\t"
        "buffer_load_dwordx4 v[220:223], %[o1], %[rs], %[sr] offen sc1\n\t"
        "buffer_load_dwordx4 v[224:227], %[o2], %[rs], %[sr] offen sc1\n\t"
        "buffer_load_dwordx4 v[228:231], %[o3], %[rs], %[sr] offen sc1\n\t"
        "buffer_load_dwordx4 v[232:235], %[o4], %[rs], %[sr] offen sc1\n\t"
        "s_add_u32 %[sr], %[sr], 196608\n\t"
        "s_cmp_eq_u32 %[sr], 1179648\n\t"
        "s_cselect_b32 %[sr], 0, %[sr]\n\t"
        DP_NOPS "\n\t"
        "buffer_load_dwordx4 v[236:239], %[o0], %[rs], %[sr] offen sc1\n\t"
        "buffer_load_dwordx4 v[240:243], %[o1], %[rs], %[sr] offen sc1\n\t"
        "buffer_load_dwordx4 v[244:247], %[o2], %[rs], %[sr] offen sc1\n\t"
        "buffer_load_dwordx4 v[248:251], %[o3], %[rs], %[sr] offen sc1\n\t"
        "buffer_load_dwordx4 v[252:255], %[o4], %[rs], %[sr] offen sc1\n\t"
        "s_add_u32 %[sr], %[sr], 196608\n\t"
        "s_cmp_eq_u32 %[sr], 1179648\n\t"
        "s_cselect_b32 %[sr], 0, %[sr]\n\t"
        "s_mov_b32 %[cnt], 0\n\t"
        "L%=_1:\n\t"
        "s_waitcnt vmcnt(14)\n\t"
        "v_mfma_f32_16x16x32_bf16 %[acc], v[196:199], %[b0], 0\n\t"
        "v_max3_u32 %[t], v196, v197, v198\n\t"
        "v_max_u32 %[t], %[t], v199\n\t"
        "s_waitcnt vmcnt(13)\n\t"
        "v_mfma_f32_16x16x32_bf16 %[acc], v[200:203], %[b1], %[acc]\n\t"
        "v_max3_u32 %[t], %[t], v200, v201\n\t"
        "v_max3_u32 %[t], %[t], v202, v203\n\t"
        "s_waitcnt vmcnt(12)\n\t"
        "v_mfma_f32_16x16x32_bf16 %[acc], v[204:207], %[b2], %[acc]\n\t"
        "v_max3_u32 %[t], %[t], v204, v205\n\t"
        "v_max3_u32 %[t], %[t], v206, v207\n\t"
        "s_waitcnt vmcnt(11)\n\t"
        "v_mfma_f32_16x16x32_bf16 %[acc], v[208:211], %[b3], %[acc]\n\t"
        "v_max3_u32 %[t], %[t], v208, v209\n\t"
        "v_max3_u32 %[t], %[t], v210, v211\n\t"
        "s_waitcnt vmcnt(10)\n\t"
        "v_mfma_f32_16x16x32_bf16 %[acc], v[212:215], %[b4], %[acc]\n\t"
        "v_max3_u32 %[t], %[t], v212, v213\n\t"
        "v_max3_u32 %[t], %[t], v214, v215\n\t"
        "v_cmp_eq_u32 vcc, -1, %[t]\n\t"
        "s_cbranch_vccz L%=_20\n\t"
        "buffer_load_dwordx4 v[196:199], %[o0], %[rs], %[sr] offen sc1\n\t"
        "buffer_load_dwordx4 v[200:203], %[o1], %[rs], %[sr] offen sc1\n\t"
        "buffer_load_dwordx4 v[204:207], %[o2], %[rs], %[sr] offen sc1\n\t"
        "buffer_load_dwordx4 v[208:211], %[o3], %[rs], %[sr] offen sc1\n\t"
        "buffer_load_dwordx4 v[212:215], %[o4], %[rs], %[sr] offen sc1\n\t"
        "s_add_u32 %[sr], %[sr], 196608\n\t"
        "s_cmp_eq_u32 %[sr], 1179648\n\t"
        "s_cselect_b32 %[sr], 0, %[sr]\n\t"
        "s_add_u32 %[cnt], %[cnt], 1\n\t"
        DP_NOPS2 "\n\t"
        "s_waitcnt vmcnt(14)\n\t"
        "v_mfma_f32_16x16x32_bf16 %[acc], v[216:219], %[b0], 0\n\t"
        "v_max3_u32 %[t], v216, v217, v218\n\t"
        "v_max_u32 %[t], %[t], v219\n\t"
        "s_waitcnt vmcnt(13)\n\t"
        "v_mfma_f32_16x16x32_bf16 %[acc], v[220:223], %[b1], %[acc]\n\t"
        "v_max3_u32 %[t], %[t], v220, v221\n\t"
        "v_max3_u32 %[t], %[t], v222, v223\n\t"
        "s_waitcnt vmcnt(12)\n\t"
        "v_mfma_f32_16x16x32_bf16 %[acc], v[224:227], %[b2], %[acc]\n\t"
        "v_max3_u32 %[t], %[t], v224, v225\n\t"
        "v_max3_u32 %[t], %[t], v226, v227\n\t"
        "s_waitcnt vmcnt(11)\n\t"
        "v_mfma_f32_16x16x32_bf16 %[acc], v[228:231], %[b3], %[acc]\n\t"
        "v_max3_u32 %[t], %[t], v228, v229\n\t"
        "v_max3_u32 %[t], %[t], v230, v231\n\t"
        "s_waitcnt vmcnt(10)\n\t"
        "v_mfma_f32_16x16x32_bf16 %[acc], v[232:235], %[b4], %[acc]\n\t"
        "v_max3_u32 %[t], %[t], v232, v233\n\t"
        "v_max3_u32 %[t], %[t], v234, v235\n\t"
        "v_cmp_eq_u32 vcc, -1, %[t]\n\t"
        "s_cbranch_vccz L%=_20\n\t"
        "buffer_load_dwordx4 v[216:219], %[o0], %[rs], %[sr] offen sc1\n\t"
        "buffer_load_dwordx4 v[220:223], %[o1], %[rs], %[sr] offen sc1\n\t"
        "buffer_load_dwordx4 v[224:227], %[o2], %[rs], %[sr] offen sc1\n\t"
        "buffer_load_dwordx4 v[228:231], %[o3], %[rs], %[sr] offen sc1\n\t"
        "buffer_load_dwordx4 v[232:235], %[o4], %[rs], %[sr] offen sc1\n\t"
        "s_add_u32 %[sr], %[sr], 196608\n\t"
        "s_cmp_eq_u32 %[sr], 1179648\n\t"
        "s_cselect_b32 %[sr], 0, %[sr]\n\t"
        "s_add_u32 %[cnt], %[cnt], 1\n\t"
        DP_NOPS2 "\n\t"
        "s_waitcnt vmcnt(14)\n\t"
        "v_mfma_f32_16x16x32_bf16 %[acc], v[236:239], %[b0], 0\n\t"
        "v_max3_u32 %[t], v236, v237, v238\n\t"
        "v_max_u32 %[t], %[t], v239\n\t"
        "s_waitcnt vmcnt(13)\n\t"
        "v_mfma_f32_16x16x32_bf16 %[acc], v[240:243], %[b1], %[acc]\n\t"
        "v_max3_u32 %[t], %[t], v240, v241\n\t"
        "v_max3_u32 %[t], %[t], v242, v243\n\t"
        "s_waitcnt vmcnt(12)\n\t"
        "v_mfma_f32_16x16x32_bf16 %[acc], v[244:247], %[b2], %[acc]\n\t"
        "v_max3_u32 %[t], %[t], v244, v245\n\t"
        "v_max3_u32 %[t], %[t], v246, v247\n\t"
        "s_waitcnt vmcnt(11)\n\t"
        "v_mfma_f32_16x16x32_bf16 %[acc], v[248:251], %[b3], %[acc]\n\t"
        "v_max3_u32 %[t], %[t], v248, v249\n\t"
        "v_max3_u32 %[t], %[t], v250, v251\n\t"
        "s_waitcnt vmcnt(10)\n\t"
        "v_mfma_f32_16x16x32_bf16 %[acc], v[252:255], %[b4], %[acc]\n\t"
        "v_max3_u32 %[t], %[t], v252, v253\n\t"
        "v_max3_u32 %[t], %[t], v254, v255\n\t"
        "v_cmp_eq_u32 vcc, -1, %[t]\n\t"
        "s_cbranch_vccz L%=_20\n\t"
        "buffer_load_dwordx4 v[236:239], %[o0], %[rs], %[sr] offen sc1\n\t"
        "buffer_load_dwordx4 v[240:243], %[o1], %[rs], %[sr] offen sc1\n\t"
        "buffer_load_dwordx4 v[244:247], %[o2], %[rs], %[sr] offen sc1\n\t"
        "buffer_load_dwordx4 v[248:251], %[o3], %[rs], %[sr] offen sc1\n\t"
        "buffer_load_dwordx4 v[252:255], %[o4], %[rs], %[sr] offen sc1\n\t"
        "s_add_u32 %[sr], %[sr], 196608\n\t"
        "s_cmp_eq_u32 %[sr], 1179648\n\t"
        "s_cselect_b32 %[sr], 0, %[sr]\n\t"
        "s_add_u32 %[cnt], %[cnt], 1\n\t"
        DP_NOPS2 "\n\t"
        "s_cmp_lt_u32 %[cnt], %[cap]\n\t"
        "s_cbranch_scc1 L%=_1\n\t"
        "s_waitcnt vmcnt(0)\n\t"
        "L%=_20:\n\t"
        "s_nop 7\n\t"
        : "=&{v[196:199]}"(A0[0]), "=&{v[200:203]}"(A0[1]), "=&{v[204:207]}"(A0[2]), "=&{v[208:211]}"(A0[3]), "=&{v[212:215]}"(A0[4]), "=&{v[216:219]}"(A1[0]), "=&{v[220:223]}"(A1[1]), "=&{v[224:227]}"(A1[2]), "=&{v[228:231]}"(A1[3]), "=&{v[232:235]}"(A1[4]), "=&{v[236:239]}"(A2[0]), "=&{v[240:243]}"(A2[1]), "=&{v[244:247]}"(A2[2]), "=&{v[248:251]}"(A2[3]), "=&{v[252:255]}"(A2[4]), [acc] "=&v"(acc), [t] "=&v"(t_), [cnt] "=&s"(cnt), [sr] "=&s"(sr)
        : [o0] "v"(aoff[0]), [o1] "v"(aoff[1]), [o2] "v"(aoff[2]), [o3] "v"(aoff[3]), [o4] "v"(aoff[4]), [b0] "v"(B[0]), [b1] "v"(B[1]), [b2] "v"(B[2]), [b3] "v"(B[3]), [b4] "v"(B[4]),
          [rs] "s"(cx.exs), [cap] "s"(SPIN_CAP)
        : "vcc", "scc", "memory");
      if (cnt >= SPIN_CAP) __hip_atomic_store(cx.ctrl + C_TMO, 1u, RLX_AGENT);
      if ((n >> 3) == (g >> 1)) *(LAS f32x4*)(RED + (((w * 2 + (g >> 1)) * 8 + (n & 7)) * 8 + 4 * (g & 1)) * 4) = acc;
    }
    BAR();
    tpub = (unsigned)__builtin_amdgcn_s_memtime();
    if (ROLE == 0) {
      const int m = l >> 3, c8 = l & 7;
      float x[8];
#pragma unroll
      for (int sl = 0; sl < 8; ++sl) x[sl] = *(LAS float*)(RED + ((sl * 8 + c8) * 8 + m) * 4);
      const float v = ((x[0] + x[1]) + (x[2] + x[3])) + ((x[4] + x[5]) + (x[6] + x[7]));
      const float o = u2f(__builtin_amdgcn_update_dpp(0, f2u(v), 0xB1, 0xf, 0xf, true));
      const unsigned d = nosent(pk2(v, o));
      u32x4 dat; dat.x = d;
      dat.y = (unsigned)__builtin_amdgcn_update_dpp(0, (int)d, 0xAA, 0xf, 0xf, true);
      dat.z = (unsigned)__builtin_amdgcn_update_dpp(0, (int)d, 0x104, 0xf, 0xf, true);
      dat.w = (unsigned)__builtin_amdgcn_update_dpp(0, (int)dat.y, 0x104, 0xf, 0xf, true);
      if (c8 == 0) {
        const unsigned ma = ex_addr((unsigned)((s + 1) & 3) * 8192u + c * 128 + m * 16, cx.pol);
#pragma unroll
        for (int r = 0; r < RM; ++r) st_sc1s(cx.ex, ma, r * 2 * MREP, dat);
      }
      st_sc1(cx.ex, ex_addr((unsigned)((s + 3) & 3) * 8192u + c * 128 + (l & 7) * 16, cx.pol) + (l >> 3) * 2 * MREP, (u32x4){SENT, SENT, SENT, SENT});
    }
    __builtin_amdgcn_s_setprio(0);
    if (ROLE == 0) asm volatile("s_waitcnt vmcnt(9)" : "+{v[196:199]}"(A0[0]), "+{v[200:203]}"(A0[1]), "+{v[204:207]}"(A0[2]), "+{v[208:211]}"(A0[3]), "+{v[212:215]}"(A0[4]), "+{v[216:219]}"(A1[0]), "+{v[220:223]}"(A1[1]), "+{v[224:227]}"(A1[2]), "+{v[228:231]}"(A1[3]), "+{v[232:235]}"(A1[4]), "+{v[236:239]}"(A2[0]), "+{v[240:243]}"(A2[1]), "+{v[244:247]}"(A2[2]), "+{v[248:251]}"(A2[3]), "+{v[252:255]}"(A2[4]) :: "memory");
    if (ROLE == 1) asm volatile("s_waitcnt vmcnt(0)" : "+{v[196:199]}"(A0[0]), "+{v[200:203]}"(A0[1]), "+{v[204:207]}"(A0[2]), "+{v[208:211]}"(A0[3]), "+{v[212:215]}"(A0[4]), "+{v[216:219]}"(A1[0]), "+{v[220:223]}"(A1[1]), "+{v[224:227]}"(A1[2]), "+{v[228:231]}"(A1[3]), "+{v[232:235]}"(A1[4]), "+{v[236:239]}"(A2[0]), "+{v[240:243]}"(A2[1]), "+{v[244:247]}"(A2[2]), "+{v[248:251]}"(A2[3]), "+{v[252:255]}"(A2[4]) :: "memory");
  }
}

__device__ __forceinline__ void col_role(const Params& p, const Ctx& cx_, LAS unsigned char* L, int c) {
  const int w = cx_.wave;
  if (w < 4) {
    const Ctx cx = fresh(cx_);
    const int tid = w * 64 + cx.lane;
    const int pc = tid & 7, sr = tid >> 3;
    st_sc1(cx.ex, ex_addr((unsigned)(sr >> 2) * MREP + (unsigned)(sr & 3) * 8192u + c * 128 + pc * 16, cx.pol), (u32x4){SENT, SENT, SENT, SENT});
  }
  {
    u32x4 B[2][8], A[4][8];
    wo_issue(p, cx_, L, 64 + c, A);
    DRAIN();
    BAR();
    wvo_part(p, cx_, L, 64 + c, A, B);
  }
  const Ctx cx = fresh(cx_);
  const int l = cx.lane, tid = w * 64 + l;
  TRG(32 + c, 1);
  if (w == 0) wait_words(cx, C_WVO, 8, 16u);
  BAR();
  TRG(32 + c, 2);
  u32x8 W[2][8];
#pragma unroll
  for (int t = 0; t < 2; ++t)
#pragma unroll
    for (int i = 0; i < 8; ++i) {
      const unsigned off = (unsigned)(OFF_WVO + (((size_t)c * 16 + 2 * w + t) * 8 + ((w + i) & 7)) * 2048) + l * 16;
      const u32x4 a = ld_sc1(cx.ws, off), b = ld_sc1(cx.ws, off + 1024);
      W[t][i] = (u32x8){a.x, a.y, a.z, a.w, b.x, b.y, b.z, b.w};
    }
  if (tid == 0) *(LAS unsigned*)(L + 8192) = 0u;
  chain_ready(cx, c);
  TRG(32 + c, 3);
  if (w == 0) col_loop<0>(p, cx, L, c, W);
  else if (w < 4) col_loop<1>(p, cx, L, c, W);
  else col_loop<2>(p, cx, L, c, W);
}

__device__ __forceinline__ float wave_sum(float v) {
#pragma unroll
  for (int m = 32; m >= 1; m >>= 1) v += __shfl_xor(v, m);
  return v;
}
__device__ __forceinline__ void ln_part(const Params& p, const Ctx& cx_, int j) {
  const Ctx cx = fresh(cx_);
  const int w = cx.wave, l = cx.lane;
  const u32x4 wr = *(const GAS u32x4*)(p.w_r + l * 8);
#pragma unroll
  for (int k = 0; k < 2; ++k) {
    const int row = 16 * j + 2 * w + k;
    const u32x4 xv = *(const GAS u32x4*)(p.x + (size_t)row * DM + l * 8);
    float v[8];
#pragma unroll
    for (int i = 0; i < 4; ++i) { v[2 * i] = bflo(xv[i]); v[2 * i + 1] = bfhi(xv[i]); }
    float s = 0.f;
#pragma unroll
    for (int i = 0; i < 8; ++i) s += v[i];
    const float mean = wave_sum(s) * (1.f / DM);
    float q = 0.f;
#pragma unroll
    for (int i = 0; i < 8; ++i) { const float d = v[i] - mean; q += d * d; }
    const float mb = bfr(mean), vb = bfr(wave_sum(q) * (1.f / DM));
    const float den = bfr(sqrtf(bfr(vb + 1e-5f)));
    float r = 0.f;
#pragma unroll
    for (int i = 0; i < 8; ++i) v[i] = bfr(bfr(v[i] - mb) / den);
#pragma unroll
    for (int i = 0; i < 4; ++i) r += v[2 * i] * bflo(wr[i]) + v[2 * i + 1] * bfhi(wr[i]);
    r = bfr(wave_sum(r));
    u32x4 o; o.x = pk2(v[0], v[1]); o.y = pk2(v[2], v[3]); o.z = pk2(v[4], v[5]); o.w = pk2(v[6], v[7]);
    st_sc1(cx.ws, (unsigned)(OFF_XN + (size_t)row * 1024) + l * 16, o);
    if (l == 0) st_sc1(cx.ws, (unsigned)(OFF_STAT + (size_t)row * 16), (u32x4){f2u(mb), f2u(den), f2u(r), 0u});
  }
  DRAIN();
  BAR();
  if (w == 0 && l == 0) arrive(cx, C_XN + (j & 7) * 32);
}
__device__ __forceinline__ void stage_w(const u16* Wsrc, int h, const Ctx& cx, LAS unsigned char* L) {
  const int w = cx.wave, l = cx.lane;
#pragma unroll
  for (int i = 0; i < 16; ++i) {
    const int row = w * 64 + i * 4 + (l >> 4), sig = l & 15;
    const int chunk = (((sig >> 1) ^ ((row & 3) << 1)) << 1) | (sig & 1);
    GLDS(Wsrc + (size_t)row * HD + h * DH + chunk * 8, L + w * 16384 + i * 1024);
  }
}
__device__ __forceinline__ void read_wt(const Ctx& cx, LAS unsigned char* L, int nt, u32x4 (&A)[32]) {
  const int l = cx.lane, hi = l >> 5, blk = (l >> 4) & 1, q = (l & 15) >> 2, pp = l & 3;
  LAS unsigned char* b0 = L + (8 * hi + q) * 256 + (((2 * nt + blk) ^ (q << 1)) * 32) + pp * 8;
#pragma unroll
  for (int u = 0; u < 32; ++u) {
    const u32x2 lo = tr_read(b0 + u * 4096), h2 = tr_read(b0 + u * 4096 + 1024);
    A[u] = (u32x4){lo.x, lo.y, h2.x, h2.y};
  }
}
constexpr int XSTR = 1040;
__device__ __forceinline__ void xn_ready(const Ctx& cx_) {
  const Ctx cx = fresh(cx_);
  if (cx.wave == 0) {
    wait_words(cx, C_XN, 8, 16u);
    __builtin_amdgcn_fence(__ATOMIC_ACQUIRE, "agent");
    DRAIN();
  }
  BAR();
}
__device__ __forceinline__ void proj_tile(const Ctx& cx, LAS unsigned char* xt, const u32x4 (&A)[32], unsigned dst) {
  const int l = cx.lane, hi = l >> 5;
  LAS unsigned char* b = xt + (l & 31) * XSTR + hi * 16;
  f32x16 acc = {0.f};
#pragma unroll
  for (int u = 0; u < 32; ++u) acc = mfma32(A[u], *(LAS u32x4*)(b + u * 32), acc);
#pragma unroll
  for (int q = 0; q < 2; ++q) {
    u32x4 o;
    o.x = pk2(acc[8 * q + 0], acc[8 * q + 1]); o.y = pk2(acc[8 * q + 2], acc[8 * q + 3]);
    o.z = pk2(acc[8 * q + 4], acc[8 * q + 5]); o.w = pk2(acc[8 * q + 6], acc[8 * q + 7]);
    st_sc1(cx.ws, dst + (2 * q + hi) * 512 + (l & 31) * 16, o);
  }
}
__device__ __forceinline__ void qk_role(const Params& p, const Ctx& cx, LAS unsigned char* L, int h, int par, int j) {
  const int w = cx.wave, l = cx.lane;
  u32x4 A[32];
  stage_w(p.Wq, h, cx, L);
  ln_part(p, cx, j);
  if (w < 4) read_wt(cx, L, w & 3, A);
  BAR();
  stage_w(p.Wk, h, cx, L);
  DRAIN();
  BAR();
  if (w >= 4) read_wt(cx, L, w & 3, A);
  xn_ready(cx);
  const u16* xn = (const u16*)(p.ws + OFF_XN);
  auto dma = [&](int s, int buf) {
#pragma unroll
    for (int i = 0; i < 4; ++i) GLDS(xn + (size_t)(32 * s + 4 * w + i) * DM + l * 8, L + buf * (32 * XSTR) + (4 * w + i) * XSTR);
  };
  dma(par, 0);
  DRAIN();
  BAR();
#pragma unroll 1
  for (int it = 0; it < 32; ++it) {
    const int s = 2 * it + par;
    if (it + 1 < 32) dma(s + 2, (it + 1) & 1);
    proj_tile(cx, L + (it & 1) * (32 * XSTR), A, (unsigned)((w < 4 ? OFF_QX : OFF_KX) + ((size_t)s * NH + h) * 8192) + (w & 3) * 2048);
    DRAIN();
    BAR();
    if (w == 0 && l == 0) arrive(cx, C_KQ + s * 32);
  }
}
__device__ __forceinline__ void v_role(const Params& p, const Ctx& cx, LAS unsigned char* L, int h, int j) {
  const int w = cx.wave, l = cx.lane;
  u32x4 A[32];
  stage_w(p.Wv, h, cx, L);
  ln_part(p, cx, j);
  read_wt(cx, L, w & 3, A);
  xn_ready(cx);
  const u16* xn = (const u16*)(p.ws + OFF_XN);
  auto dma = [&](int T, int buf) {
#pragma unroll
    for (int i = 0; i < 8; ++i) GLDS(xn + (size_t)(64 * T + 8 * w + i) * DM + l * 8, L + buf * (64 * XSTR) + (8 * w + i) * XSTR);
  };
  dma(0, 0);
  DRAIN();
  BAR();
#pragma unroll 1
  for (int T = 0; T < 32; ++T) {
    if (T + 1 < 32) dma(T + 1, (T + 1) & 1);
    const int s = 2 * T + (w >> 2);
    proj_tile(cx, L + (T & 1) * (64 * XSTR) + (w >> 2) * (32 * XSTR), A, (unsigned)(OFF_VX + ((size_t)s * NH + h) * 8192) + (w & 3) * 2048);
    DRAIN();
    BAR();
    if (w == 0 && l == 0) __hip_atomic_store(cx.ctrl + C_V + h * 32, (unsigned)T, RLX_AGENT);
  }
}
__device__ __forceinline__ void u_role(const Params& p, const Ctx& cx_, LAS unsigned char* L, int h, int j) {
  u32x4 B[2][8];
  {
    u32x4 A[4][8];
    wo_issue(p, cx_, L, h, A);
    ln_part(p, cx_, j);
    wvo_part(p, cx_, L, h, A, B);
  }
  const Ctx cx = fresh(cx_);
  const int w = cx.wave, l = cx.lane, hi = l >> 5;
#pragma unroll 1
  for (int T = 0; T < 32; ++T) {
    TRU(0);
    wait_words(cx, C_V + h * 32, 1, (unsigned)(T + 1));
    TRU(1);
    u32x4 A[2][8];
#pragma unroll
    for (int k = 0; k < 2; ++k)
#pragma unroll
      for (int u = 0; u < 8; ++u) A[k][u] = ld_sc1(cx.ws, (unsigned)(OFF_VX + ((size_t)(2 * T + k) * NH + h) * 8192) + (2 * u + hi) * 512 + (l & 31) * 16);
#pragma unroll
    for (int k = 0; k < 2; ++k) {
#pragma unroll
      for (int nt = 0; nt < 2; ++nt) {
        f32x16 acc = {0.f};
#pragma unroll
        for (int u = 0; u < 8; ++u) acc = mfma32(A[k][u], B[nt][u], acc);
        const int col = 64 * w + 32 * nt + (l & 31);
        const unsigned base = (unsigned)(OFF_U + ((size_t)(2 * T + k) * NCOL + (col >> 3)) * U_SC) + ((h * 5 + hi) * 8 + (col & 7)) * 16;
        u32x4 o;
        o.x = pk2(acc[0], acc[1]); o.y = pk2(acc[2], acc[3]); o.z = pk2(acc[8], acc[9]); o.w = pk2(acc[10], acc[11]);
        st_sc1(cx.ws, base, o);
        o.x = pk2(acc[4], acc[5]); o.y = pk2(acc[6], acc[7]); o.z = pk2(acc[12], acc[13]); o.w = pk2(acc[14], acc[15]);
        st_sc1(cx.ws, base + 256, o);
      }
    }
    TRU(2);
    DRAIN();
    BAR();
    TRU(3);
    if (w == 0 && l < 2) arrive(cx, C_U + (2 * T + l) * 32);
  }
}

__device__ __forceinline__ unsigned long long umax64(unsigned long long a, unsigned long long b) { return a > b ? a : b; }
__device__ __forceinline__ void to_role(const Params& p, const Ctx& cx_, LAS unsigned char* L, int x, int f) {
  const int t32 = x * 8 + f;
  {
    u32x4 B[2][8], A[4][8];
    wo_issue(p, cx_, L, 32 + t32, A);
    DRAIN();
    BAR();
    wvo_part(p, cx_, L, 32 + t32, A, B);
  }
  const Ctx cx = fresh(cx_);
  const int w = cx.wave, l = cx.lane, tid = w * 64 + l, hi = l >> 5, g = l >> 4, n = l & 15;
  TRG(96 + t32, 2);
  LAS int* IDX = (LAS int*)(L + 140 * 1024);
  LAS unsigned long long* WK = (LAS unsigned long long*)(L + 140 * 1024 + 64);
  LAS float* UX = (LAS float*)(L + 141 * 1024);
  if (t32 < 28) {
    const u16* wsrc = (w < 4 ? p.w_gate : p.w_up) + 64 * t32 + (l & 7) * 8;
#pragma unroll
    for (int i = 0; i < 16; ++i) {
      const int blk = (w & 3) * 16 + i;
      GLDS(wsrc + (size_t)(8 * blk + (l >> 3)) * FF, L + (w >> 2) * 65536 + blk * 1024);
    }
  }
  if (w == 0) wait_words(cx, C_XN, 8, 16u);
  BAR();
  {
    unsigned long long key[4];
#pragma unroll
    for (int i = 0; i < 4; ++i) {
      const int row = tid + 512 * i;
      const unsigned rb = __builtin_amdgcn_raw_buffer_load_b32(cx.ws, (unsigned)(OFF_STAT + (size_t)row * 16 + 8), 0, AUX_SC1);
      const unsigned mono = (rb & 0x80000000u) ? ~rb : (rb | 0x80000000u);
      key[i] = ((unsigned long long)mono << 32) | (unsigned)(0xFFFF - row);
    }
#pragma unroll 1
    for (int c = 0; c < TOPC; ++c) {
      unsigned long long b = umax64(umax64(key[0], key[1]), umax64(key[2], key[3]));
#pragma unroll
      for (int m = 32; m >= 1; m >>= 1) b = umax64(b, __shfl_xor(b, m));
      if (l == 0) WK[(c & 1) * 8 + w] = b;
      BAR();
      b = WK[(c & 1) * 8];
#pragma unroll
      for (int i = 1; i < 8; ++i) b = umax64(b, WK[(c & 1) * 8 + i]);
#pragma unroll
      for (int i = 0; i < 4; ++i) if (key[i] == b) key[i] = 0ull;
      if (tid == 0) {
        const unsigned mono = (unsigned)(b >> 32);
        const float r = u2f((mono & 0x80000000u) ? (mono & 0x7fffffffu) : ~mono);
        IDX[c] = 0xFFFF - (int)(b & 0xFFFFu);
        IDX[8 + c] = (int)f2u(bfr(1.f / (1.f + __expf(-r))));
      }
    }
  }
  DRAIN();
  BAR();
  const int q4 = n >> 2, pp = l & 3;
  if (t32 < 28) {
    const unsigned xb = (unsigned)(OFF_XN + (size_t)IDX[l & 7] * 1024) + g * 16;
    u32x4 A[16];
#pragma unroll
    for (int u = 0; u < 16; ++u) A[u] = ld_sc1(cx.ws, xb + u * 64);
    LAS unsigned char* b0 = L + (w >> 2) * 65536 + (8 * g + q4) * 128 + (w & 3) * 32 + pp * 8;
    f32x4 acc = {0.f, 0.f, 0.f, 0.f};
#pragma unroll
    for (int u = 0; u < 16; ++u) {
      const u32x2 lo = tr_read(b0 + u * 4096), h2 = tr_read(b0 + u * 4096 + 512);
      acc = mfma16(A[u], (u32x4){lo.x, lo.y, h2.x, h2.y}, acc);
    }
    if (w >= 4 && g < 2) {
#pragma unroll
      for (int r = 0; r < 4; ++r) UX[(4 * g + r) * 64 + (w & 3) * 16 + n] = bfr(acc[r]);
    }
    BAR();
    LAS u16* GT = (LAS u16*)(L + 144 * 1024);
    if (w < 4 && g < 2) {
#pragma unroll
      for (int r = 0; r < 4; ++r) {
        const float gs = bfr(acc[r]);
        const float sg = bfr(gs / (1.f + __expf(-gs)));
        GT[(4 * g + r) * 64 + w * 16 + n] = f2bf(sg * UX[(4 * g + r) * 64 + w * 16 + n]);
      }
    }
    BAR();
    if (w == 0) {
      st_sc1(cx.ws, (unsigned)(OFF_GU + (size_t)(l >> 3) * (FF * 2) + t32 * 128 + (l & 7) * 16), *(LAS u32x4*)((LAS unsigned char*)GT + l * 16));
      DRAIN();
      if (l == 0) arrive(cx, C_GU);
    }
  }
  {
    const u16* wsrc = p.w_down + 16 * t32 + (l & 1) * 8;
#pragma unroll
    for (int i = 0; i < 7; ++i) {
      const int blk = w * 7 + i;
      GLDS(wsrc + (size_t)(32 * blk + (l >> 1)) * DM, L + blk * 1024);
    }
    if (w == 0) wait_words(cx, C_GU, 1, 28u);
    DRAIN();
    BAR();
    const unsigned gb = (unsigned)(OFF_GU + (size_t)(l & 7) * (FF * 2)) + (7 * w) * 64 + g * 16;
    u32x4 A[7];
#pragma unroll
    for (int u = 0; u < 7; ++u) A[u] = ld_sc1(cx.ws, gb + u * 64);
    LAS unsigned char* b0 = L + (7 * w) * 1024 + (8 * g + q4) * 32 + pp * 8;
    f32x4 acc = {0.f, 0.f, 0.f, 0.f};
#pragma unroll
    for (int u = 0; u < 7; ++u) {
      const u32x2 lo = tr_read(b0 + u * 1024), h2 = tr_read(b0 + u * 1024 + 128);
      acc = mfma16(A[u], (u32x4){lo.x, lo.y, h2.x, h2.y}, acc);
    }
    LAS float* PR = (LAS float*)(L + 65536);
    if (g < 2) {
#pragma unroll
      for (int r = 0; r < 4; ++r) PR[(w * 8 + 4 * g + r) * 16 + n] = acc[r];
    }
    BAR();
    if (w < 2) {
      const int r = tid >> 4;
      float y = 0.f;
#pragma unroll
      for (int k = 0; k < 8; ++k) y += PR[(k * 8 + r) * 16 + n];
      st_sc1_32(cx.ws, (unsigned)(OFF_Y + (size_t)(r * DM + 16 * t32 + n) * 4), f2u(bfr(bfr(y) * u2f((unsigned)IDX[8 + r]))));
      DRAIN();
    }
    BAR();
    if (tid == 0) arrive(cx, C_Y);
  }
  TRG(96 + t32, 3);
  const int hf = f & 1;
  const int cb0 = x * 16 + hf * 8;
  LAS float* RD = (LAS float*)L;
  bool first = true;
#pragma unroll 1
  for (int s = f >> 1; s < NSEG; s += 4) {
    TRT(0);
    if (w == 0) { wait_words(cx, C_U + s * 32, 1, 32u); wait_words(cx, C_KQ + s * 32, 1, 32u); }
    BAR();
    TRT(1);
    const unsigned ub = (unsigned)(OFF_U + ((size_t)s * NCOL + cb0 + ((l & 31) >> 3)) * U_SC) + (l & 7) * 16 + w * (4 * 5 * 128);
    u32x4 Bx[2][8];
#pragma unroll
    for (int nt = 0; nt < 2; ++nt)
#pragma unroll
      for (int u = 0; u < 8; ++u) Bx[nt][u] = ld_sc1(cx.ws, ub + nt * (4 * (unsigned)U_SC) + ((u >> 1) * 5 + 2 * (u & 1) + hi) * 128);
    {
      u32x4 t[8];
      const unsigned kb = (unsigned)(((size_t)s * NH + x * 8 + hf * 4) * 8192) + tid * 16;
#pragma unroll
      for (int i = 0; i < 4; ++i) { t[i] = ld_sc1(cx.ws, (unsigned)OFF_KX + kb + i * 8192); t[4 + i] = ld_sc1(cx.ws, (unsigned)OFF_QX + kb + i * 8192); }
#pragma unroll
      for (int i = 0; i < 8; ++i) asm volatile("" ::"v"(t[i]));
    }
    TRT(2);
    const int orow = 32 * s + (tid >> 4), ocol = 128 * x + 64 * hf + (tid & 15) * 4;
    const u32x2 xin = *(const GAS u32x2*)(p.x + (size_t)orow * DM + ocol);
    const u32x2 stat = __builtin_amdgcn_raw_buffer_load_b64(cx.ws, (unsigned)(OFF_STAT + (size_t)orow * 16), 0, AUX_SC1);
    if (w == 0) {
      if (first) wait_words(cx, C_Y, 1, 32u);
      gu32* a = cx.ctrl + (l < 32 ? C_FP + l * 32 : C_FUM + (cb0 + (l & 7)) * 32);
      for (unsigned spins = 0;; ++spins) {
        const unsigned v = __hip_atomic_load(a, RLX_AGENT) + 1u;
        if (__all(v >= (unsigned)(s + 1))) break;
        if (spins > WAIT_CAP) { __hip_atomic_store(cx.ctrl + C_TMO, 1u, RLX_AGENT); break; }
        if ((spins & 63u) == 63u && __builtin_amdgcn_readfirstlane(__hip_atomic_load(cx.ctrl + C_TMO, RLX_AGENT)) == 1u) break;
        __builtin_amdgcn_s_sleep(TO_SLEEP);
      }
    }
    first = false;
    BAR();
    TRT(3);
    const unsigned pb = (unsigned)(OFF_PX + ((size_t)s * NH + w * 4) * 2560) + (l & 31) * 16;
    u32x4 Ax[8], Am[2], Bm[2][2];
#pragma unroll
    for (int u = 0; u < 8; ++u) Ax[u] = ld_sc1(cx.ws, pb + ((u >> 1) * 5 + 2 * (u & 1) + hi) * 512);
#pragma unroll
    for (int v = 0; v < 2; ++v) {
      Am[v] = ld_sc1(cx.ws, pb + ((2 * v + hi) * 5 + 4) * 512);
      Bm[0][v] = ld_sc1(cx.ws, ub + ((2 * v + hi) * 5 + 4) * 128);
      Bm[1][v] = ld_sc1(cx.ws, ub + 4 * (unsigned)U_SC + ((2 * v + hi) * 5 + 4) * 128);
    }
    int sel = -1;
#pragma unroll
    for (int c = 0; c < TOPC; ++c) if (IDX[c] == orow) sel = c;
    f32x4 up4 = {0.f, 0.f, 0.f, 0.f};
    if (sel >= 0) up4 = __builtin_bit_cast(f32x4, ld_sc1(cx.ws, (unsigned)(OFF_Y + (size_t)(sel * DM + ocol) * 4)));
#pragma unroll
    for (int nt = 0; nt < 2; ++nt) {
      f32x16 acc = {0.f};
#pragma unroll
      for (int u = 0; u < 8; ++u) acc = mfma32(Ax[u], Bx[nt][u], acc);
#pragma unroll
      for (int v = 0; v < 2; ++v) acc = mfma32(Am[v], Bm[nt][v], acc);
#pragma unroll
      for (int r = 0; r < 16; ++r) RD[(w * 32 + (r & 3) + 8 * (r >> 2) + 4 * hi) * 68 + nt * 32 + (l & 31)] = acc[r];
    }
    BAR();
    TRT(4);
    {
      LAS float* q = RD + (tid >> 4) * 68 + (tid & 15) * 4;
      f32x4 sum = *(LAS f32x4*)q;
#pragma unroll
      for (int k = 1; k < 8; ++k) sum += *(LAS f32x4*)(q + k * 32 * 68);
      const float mb = u2f(stat.x), den = u2f(stat.y);
      float o[4];
#pragma unroll
      for (int k = 0; k < 4; ++k) {
        const float xv = (k & 1) ? bfhi(xin[k >> 1]) : bflo(xin[k >> 1]);
        const float xnv = bfr(bfr(xv - mb) / den);
        o[k] = bfr(xv + bfr(sum[k])) + bfr(xnv + up4[k]);
      }
      u32x2 ov; ov.x = pk2(o[0], o[1]); ov.y = pk2(o[2], o[3]);
      *(GAS u32x2*)(p.out + (size_t)orow * DM + ocol) = ov;
    }
  }
}

constexpr int LDS_BYTES = 156 * 1024;

__global__ void __launch_bounds__(512, 2) k_fused(Params p) {
  extern __shared__ __attribute__((aligned(16))) unsigned char lds_raw[];
  LAS unsigned char* L = (LAS unsigned char*)lds_raw;
  const int tid = threadIdx.x;
  Ctx cx;
  cx.wave = __builtin_amdgcn_readfirstlane(tid >> 6); cx.lane = tid & 63;
  cx.ctrl = (gu32*)p.ws;
  const uintptr_t exb = ((uintptr_t)p.ws + CTRL_BYTES + 16383) & ~(uintptr_t)16383;
  cx.ex = __builtin_amdgcn_make_buffer_rsrc((void*)exb, 0, (int)EX_BYTES, 0x00020000);
  cx.exs = (u32x4){(unsigned)exb, (unsigned)(exb >> 32) & 0xffffu, (unsigned)EX_BYTES, 0x00020000u};
  cx.ws = __builtin_amdgcn_make_buffer_rsrc((void*)p.ws, 0, (int)WS_END, 0x00020000);
  cx.pol = 0;
  volatile LAS int* sh = (volatile LAS int*)(L + 155 * 1024);
  if (tid == 0) {
    const unsigned x = xcc_id() & 7u;
    const unsigned t = __hip_atomic_fetch_add(cx.ctrl + C_RC + x * 32, 1u, RLX_AGENT) + 1u;
    __hip_atomic_fetch_add(cx.ctrl + C_TOT, 1u, RLX_AGENT);
    int role;
    if (t < 32u) role = (int)(x * 32 + t);
    else {
      const unsigned o = __hip_atomic_fetch_add(cx.ctrl + C_OVF, 1u, RLX_AGENT) + 1u;
      unsigned spins = 0; role = -1;
      while (__hip_atomic_load(cx.ctrl + C_TOT, RLX_AGENT) + 1u < 256u) { if (++spins > WAIT_CAP) break; __builtin_amdgcn_s_sleep(2); }
      unsigned seen = 0;
      for (int xx = 0; xx < 8 && role < 0; ++xx) {
        const unsigned cnt = __hip_atomic_load(cx.ctrl + C_RC + xx * 32, RLX_AGENT) + 1u;
        const unsigned holes = cnt < 32u ? 32u - cnt : 0u;
        if (o < seen + holes) role = xx * 32 + (int)(cnt + (o - seen));
        seen += holes;
      }
    }
    sh[0] = role;
  }
  __syncthreads();
  const int role = __builtin_amdgcn_readfirstlane(sh[0]);
  if (role < 0) return;
  TRG(role < 128 ? ((role & 31) < 8 ? (role >> 5) * 8 + (role & 31) : ((role & 31) < 24 ? 32 + (role >> 5) * 16 + (role & 31) - 8 : 96 + (role >> 5) * 8 + (role & 31) - 24)) : role, 0);
  const int rx = role >> 5, rt_ = role & 31;
  if (rx >= 4) {
    const int j = role - 128, h = j >> 2, k = j & 3;
    if (k < 2) qk_role(p, cx, L, h, k, j);
    else if (k == 2) v_role(p, cx, L, h, j);
    else u_role(p, cx, L, h, j);
    TRG(role, 1);
    return;
  }
  if (rt_ >= 24) { to_role(p, cx, L, rx, rt_ - 24); TRG(96 + rx * 8 + rt_ - 24, 1); return; }
  if (role == 0 && cx.wave == 0) {
    unsigned best0 = ~0u, best1 = ~0u;
    for (int rep = 0; rep < 4; ++rep) {
      unsigned t0 = now32();
      if (cx.lane == 0) st_sc1_32(cx.ex, TAGU * 2, 0u);
      DRAIN();
      unsigned t1 = now32();
      best0 = min(best0, t1 - t0);
      if (cx.lane == 0) st_sc1_32(cx.ex, TAGU * 2 + 8192u, 0u);
      DRAIN();
      t0 = now32();
      best1 = min(best1, t0 - t1);
    }
    if (cx.lane == 0) __hip_atomic_store(cx.ctrl + C_POL, best1 < best0 ? 1u : 0u, RLX_AGENT);
  }
  if (cx.wave == 0) {
    wait_words(cx, C_POL, 1, 1u);
    if (cx.lane == 0) sh[1] = (int)(__hip_atomic_load(cx.ctrl + C_POL, RLX_AGENT) & 1u);
  }
  __syncthreads();
  cx.pol = (unsigned)__builtin_amdgcn_readfirstlane(sh[1]);
  if (rt_ < 8) head_role(p, cx, L, rx * 8 + rt_);
  else col_role(p, cx, L, rx * 16 + rt_ - 8);
}

extern "C" void kernel_launch(void* const* d_in, const int* in_sizes, int n_in, void* d_out, int out_size, void* d_ws, size_t ws_size, hipStream_t stream) {
  Params p;
  p.x = (const u16*)d_in[0]; p.mem0 = (const u16*)d_in[1]; p.Wq = (const u16*)d_in[2]; p.Wk = (const u16*)d_in[3]; p.Wv = (const u16*)d_in[4]; p.Wo = (const u16*)d_in[5];
  p.w_r = (const u16*)d_in[8]; p.w_gate = (const u16*)d_in[9]; p.w_up = (const u16*)d_in[10]; p.w_down = (const u16*)d_in[11];
  p.out = (u16*)d_out; p.ws = (unsigned char*)d_ws;
  p.dh = D_H; p.dm = D_M; p.dp = D_P; p.pad = 0;
  (void)hipFuncSetAttribute((const void*)k_fused, hipFuncAttributeMaxDynamicSharedMemorySize, LDS_BYTES);
  (void)hipMemsetAsync(d_ws, 0xFF, CTRL_BYTES, stream);
  k_fused<<<256, 512, LDS_BYTES, stream>>>(p);
}
```

```cpp
#include <hip/hip_runtime.h>
#include <hip/hip_bf16.h>
#include <stdint.h>

#define GAS __attribute__((address_space(1)))
#define LAS __attribute__((address_space(3)))
typedef unsigned short u16;
typedef __bf16 bf16x8 __attribute__((ext_vector_type(8)));
typedef __bf16 bf16x16 __attribute__((ext_vector_type(16)));
typedef __bf16 bf16x2 __attribute__((ext_vector_type(2)));
typedef float f32x4 __attribute__((ext_vector_type(4)));
typedef float f32x2 __attribute__((ext_vector_type(2)));
typedef float f32x16 __attribute__((ext_vector_type(16)));
typedef unsigned u32x4 __attribute__((ext_vector_type(4)));
typedef unsigned u32x2 __attribute__((ext_vector_type(2)));
typedef unsigned u32x8 __attribute__((ext_vector_type(8)));
typedef GAS unsigned gu32;
#define RLX_AGENT __ATOMIC_RELAXED, __HIP_MEMORY_SCOPE_AGENT

constexpr int S_ = 2048, DM = 512, NH = 32, DH = 128, HD = 4096, NSEG = 64, FF = 1792, TOPC = 8;
constexpr int NCOL = 64;

constexpr size_t CTRL_BYTES = 131072;
constexpr int RM = 4, RP = 3;
constexpr unsigned MREP = 4 * 8192u, PBASE = RM * MREP, PSLOT = 3 * 8192u, PREP = 4 * PSLOT, TAGU = PBASE + RP * PREP;
constexpr unsigned FPU = TAGU + 8192u, FUMU = TAGU + 16384u;
constexpr size_t EX_BYTES = (size_t)(TAGU + 3 * 8192u) * 2;
constexpr size_t OFF_XN = 4u << 20;
constexpr size_t OFF_KX = OFF_XN + (size_t)S_ * DM * 2;
constexpr size_t OFF_QX = OFF_KX + (size_t)S_ * HD * 2;
constexpr size_t OFF_VX = OFF_QX + (size_t)S_ * HD * 2;
constexpr size_t OFF_U = OFF_VX + (size_t)S_ * HD * 2;
constexpr size_t U_SC = 160 * 128;
constexpr size_t OFF_WVO = OFF_U + (size_t)NSEG * NCOL * U_SC;
constexpr size_t OFF_PX = OFF_WVO + (size_t)NCOL * 16 * 8 * 64 * 32;
constexpr size_t OFF_STAT = OFF_PX + (size_t)NSEG * NH * 5 * 512;
constexpr size_t OFF_GU = OFF_STAT + (size_t)S_ * 16;
constexpr size_t OFF_Y = OFF_GU + TOPC * FF * 4;
constexpr size_t OFF_DBG = OFF_Y + TOPC * DM * 4;
constexpr size_t WS_END = OFF_DBG + (1u << 20);

constexpr int C_RC = 0, C_OVF = 256, C_TOT = 288, C_POL = 320, C_TMO = 352, C_GU = 384, C_Y = 416;
constexpr int C_XN = 512, C_WVO = 768, C_RDY = 1024;
constexpr int C_V = 2048;
constexpr int C_KQ = 4096, C_U = 6144;
constexpr int C_FP = 8192, C_FUM = 9216;

#define D_H 3450
#define D_M 600
#define D_P 1600

#define TO_SLEEP 8
#define COL_HSLEEP 4
struct Params {
  const u16 *x, *mem0, *Wq, *Wk, *Wv, *Wo, *w_r, *w_gate, *w_up, *w_down;
  u16* out;
  unsigned char* ws;
  int dh, dm, dp, pad;
};

__device__ __forceinline__ unsigned f2u(float f) { return __builtin_bit_cast(unsigned, f); }
__device__ __forceinline__ float u2f(unsigned u) { return __builtin_bit_cast(float, u); }
__device__ __forceinline__ float bf2f(u16 b) { return u2f((unsigned)b << 16); }
__device__ __forceinline__ float bflo(unsigned u) { return u2f(u << 16); }
__device__ __forceinline__ float bfhi(unsigned u) { return u2f(u & 0xffff0000u); }
__device__ __forceinline__ u16 f2bf(float f) { __bf16 b = (__bf16)f; return __builtin_bit_cast(u16, b); }
__device__ __forceinline__ float bfr(float f) { return bf2f(f2bf(f)); }
__device__ __forceinline__ unsigned pk2(float lo, float hi) { f32x2 v = {lo, hi}; bf16x2 b = __builtin_convertvector(v, bf16x2); return __builtin_bit_cast(unsigned, b); }

constexpr int AUX_SC1 = 16;
constexpr unsigned SENT = 0xFFFFFFFFu;
constexpr unsigned SPIN_CAP = 1u << 15;
constexpr unsigned WAIT_CAP = 1u << 17;
__device__ __forceinline__ unsigned xcc_id() { return (unsigned)__builtin_amdgcn_s_getreg(20 | (3 << 11)) & 0xfu; }
__device__ __forceinline__ unsigned now32() { unsigned long long t; asm volatile("s_memtime %0\n\ts_waitcnt lgkmcnt(0)" : "=s"(t)::"memory"); return (unsigned)t; }
__device__ __forceinline__ u32x4 ld_sc1(__amdgpu_buffer_rsrc_t rs, unsigned off) { return __builtin_amdgcn_raw_buffer_load_b128(rs, off, 0, AUX_SC1); }
__device__ __forceinline__ void st_sc1(__amdgpu_buffer_rsrc_t rs, unsigned off, u32x4 v) { __builtin_amdgcn_raw_buffer_store_b128(v, rs, off, 0, AUX_SC1); }
__device__ __forceinline__ void st_sc1s(__amdgpu_buffer_rsrc_t rs, unsigned off, unsigned soff, u32x4 v) { __builtin_amdgcn_raw_buffer_store_b128(v, rs, off, soff, AUX_SC1); }
__device__ __forceinline__ void st_sc1_64(__amdgpu_buffer_rsrc_t rs, unsigned off, u32x2 v) { __builtin_amdgcn_raw_buffer_store_b64(v, rs, off, 0, AUX_SC1); }
__device__ __forceinline__ void st_sc1_32(__amdgpu_buffer_rsrc_t rs, unsigned off, unsigned v) { __builtin_amdgcn_raw_buffer_store_b32(v, rs, off, 0, AUX_SC1); }
__device__ __forceinline__ unsigned nosent(unsigned x) { return min(x, 0xFFFFFFFEu); }
__device__ __forceinline__ f32x4 mfma16(u32x4 a, u32x4 b, f32x4 c) { return __builtin_amdgcn_mfma_f32_16x16x32_bf16(__builtin_bit_cast(bf16x8, a), __builtin_bit_cast(bf16x8, b), c, 0, 0, 0); }
__device__ __forceinline__ f32x16 mfma32(u32x4 a, u32x4 b, f32x16 c) { return __builtin_amdgcn_mfma_f32_32x32x16_bf16(__builtin_bit_cast(bf16x8, a), __builtin_bit_cast(bf16x8, b), c, 0, 0, 0); }
__device__ __forceinline__ f32x4 smfma(u32x4 a, u32x8 b, f32x4 c, int idx) { return __builtin_amdgcn_smfmac_f32_16x16x64_bf16(__builtin_bit_cast(bf16x8, a), __builtin_bit_cast(bf16x16, b), c, idx, 0, 0); }
typedef short v4i16_t __attribute__((ext_vector_type(4)));
__device__ __forceinline__ u32x2 tr_read(LAS unsigned char* p) { return __builtin_bit_cast(u32x2, __builtin_amdgcn_ds_read_tr16_b64_v4i16((LAS v4i16_t*)p)); }
__device__ __forceinline__ unsigned ex_addr(unsigned i, unsigned pol) { return (i >> 13) * 16384u + (i & 8191u) + pol * 8192u; }
__device__ __forceinline__ unsigned dpp_ror8(unsigned x) { return (unsigned)__builtin_amdgcn_update_dpp(0, (int)x, 0x128, 0xf, 0xf, true); }
#define BAR() do { asm volatile("" ::: "memory"); __builtin_amdgcn_s_waitcnt(0xC07F); __builtin_amdgcn_s_barrier(); asm volatile("" ::: "memory"); } while (0)
#define DRAIN() do { asm volatile("s_waitcnt vmcnt(0)" ::: "memory"); __builtin_amdgcn_s_waitcnt(0x0F70); } while (0)
#define GLDS(src, dst) __builtin_amdgcn_global_load_lds((const GAS unsigned*)(src), (LAS unsigned*)(dst), 16, 0, 0)
#define DM_NOPS "s_nop 15\n\ts_nop 15"
#define DM_NOPS2 "s_nop 7"
#define DP_NOPS "s_nop 15\n\ts_nop 15\n\ts_nop 15\n\ts_nop 7"
#define DP_NOPS2 "s_nop 15"

#define TRG(id, k) do {} while (0)
#define TRT(k) do {} while (0)
#define TRU(k) do {} while (0)

#define TR(k) do {} while (0)
#define TR_CAL(id, o) do {} while (0)
#define TR_OUT(id) do {} while (0)
struct Ctx {
  __amdgpu_buffer_rsrc_t ex;
  u32x4 exs;
  __amdgpu_buffer_rsrc_t ws;
  gu32* ctrl;
  unsigned pol;
  int wave, lane;
};

__device__ __forceinline__ int lane_id() { int l; asm volatile("v_mbcnt_lo_u32_b32 %0, -1, 0\n\tv_mbcnt_hi_u32_b32 %0, -1, %0" : "=v"(l)); return l; }
__device__ __forceinline__ Ctx fresh(const Ctx& c) { Ctx r = c; r.lane = lane_id(); return r; }
__device__ __forceinline__ void arrive(const Ctx& cx, int word) { __hip_atomic_fetch_add(cx.ctrl + word, 1u, RLX_AGENT); }
__device__ __forceinline__ void wait_words(const Ctx& cx, int word, int n, unsigned want) {
  gu32* a = cx.ctrl + word + (cx.lane < n ? cx.lane : 0) * 32;
  for (unsigned spins = 0;; ++spins) {
    const unsigned v = __hip_atomic_load(a, RLX_AGENT) + 1u;
    if (__all(v >= want)) break;
    if (spins > WAIT_CAP) { __hip_atomic_store(cx.ctrl + C_TMO, 1u, RLX_AGENT); break; }
    if ((spins & 255u) == 255u && __builtin_amdgcn_readfirstlane(__hip_atomic_load(cx.ctrl + C_TMO, RLX_AGENT)) == 1u) break;
    __builtin_amdgcn_s_sleep(2);
  }
  __builtin_amdgcn_fence(__ATOMIC_ACQUIRE, "wavefront");
}
__device__ __forceinline__ void timed_wait(unsigned tref, int d) {
  while ((int)(now32() - tref) < d - 200) __builtin_amdgcn_s_sleep(1);
  while ((int)(now32() - tref) < d) {}
}

template <class HOOK>
__device__ __forceinline__ u32x2 front(const Params& p, const Ctx& cx, LAS unsigned char* MEMIMG, const u32x8 (&W)[2][8], int s, unsigned& tref, HOOK hook) {
  const int w = cx.wave, l = cx.lane;
  LAS unsigned char* L = MEMIMG;
  TR(0);
  u32x4 v, v1 = {0, 0, 0, 0}, v2 = v1, v3 = v1, v0 = v1;
  if (s == 0) {
    const u16* mp = p.mem0 + (l & 7) * DM + (8 * w + (l >> 3)) * 8;
    asm volatile("global_load_dwordx4 %0, %1, off\n\ts_waitcnt vmcnt(0)" : "=&{v[236:239]}"(v) : "v"(mp) : "memory");
  } else {
    const unsigned off = ex_addr((unsigned)(s & 3) * 8192u + w * 1024 + l * 16, cx.pol);
    unsigned t_, cnt, sr;
    asm volatile(
      "s_mov_b32 %[sr], 0\n\t"
      "buffer_load_dwordx4 v[240:243], %[off], %[rs], %[sr] offen sc1\n\t"
      "s_add_u32 %[sr], %[sr], 65536\n\t"
      "s_cmp_eq_u32 %[sr], 262144\n\t"
      "s_cselect_b32 %[sr], 0, %[sr]\n\t"
      "s_mov_b32 %[cnt], 0\n\t"
      "L%=_1:\n\t"
      "s_waitcnt vmcnt(0)\n\t"
      "v_max3_u32 %[t], v240, v241, v242\n\t"
      "v_max_u32 %[t], %[t], v243\n\t"
      "v_cmp_eq_u32 vcc, -1, %[t]\n\t"
      "s_cbranch_vccz L%=_10\n\t"
      "buffer_load_dwordx4 v[240:243], %[off], %[rs], %[sr] offen sc1\n\t"
      "s_add_u32 %[sr], %[sr], 65536\n\t"
      "s_cmp_eq_u32 %[sr], 262144\n\t"
      "s_cselect_b32 %[sr], 0, %[sr]\n\t"
      "s_add_u32 %[cnt], %[cnt], 1\n\t"
      DM_NOPS2 "\n\t"
      "s_cmp_lt_u32 %[cnt], %[cap]\n\t"
      "s_cbranch_scc1 L%=_1\n\t"
      "s_waitcnt vmcnt(0)\n\t"
      "L%=_10:\n\t"
      "v_mov_b32 v236, v240\n\t"
      "v_mov_b32 v237, v241\n\t"
      "v_mov_b32 v238, v242\n\t"
      "v_mov_b32 v239, v243\n\t"
      "L%=_20:\n\t"
      : "=&{v[240:243]}"(v0), "=&{v[244:247]}"(v1), "=&{v[248:251]}"(v2), "=&{v[252:255]}"(v3), "=&{v[236:239]}"(v), [t] "=&v"(t_), [cnt] "=&s"(cnt), [sr] "=&s"(sr)
      : [off] "v"(off), [rs] "s"(cx.exs), [cap] "s"(SPIN_CAP)
      : "vcc", "scc", "memory");
    if (cnt >= SPIN_CAP) __hip_atomic_store(cx.ctrl + C_TMO, 1u, RLX_AGENT);
  }
  TR(1);
  LAS unsigned char* mb = MEMIMG + l * 16;
  *(LAS u32x4*)(mb + w * 1024) = v;
  const int idx = (l & 8) ? 0xEEEE : 0x4444;
  f32x4 a0 = {0.f, 0.f, 0.f, 0.f}, a1 = {0.f, 0.f, 0.f, 0.f};
  a0 = smfma(v, W[0][0], a0, idx);
  a1 = smfma(v, W[1][0], a1, idx);
  asm volatile("" : "+v"(a0), "+v"(a1));
  BAR();
  TR(2);
  u32x4 a[7];
#pragma unroll
  for (int i = 1; i < 8; ++i) a[i - 1] = *(LAS u32x4*)(mb + ((w + i) & 7) * 1024);
  __builtin_amdgcn_sched_barrier(0);
#pragma unroll
  for (int i = 1; i < 8; ++i) {
    a0 = smfma(a[i - 1], W[0][i], a0, idx);
    a1 = smfma(a[i - 1], W[1][i], a1, idx);
    if (i == 5) {
      asm volatile("" : "+v"(a0), "+v"(a1));
      tref = now32();
      hook();
      __builtin_amdgcn_sched_barrier(0);
    }
  }
  float o[4];
#pragma unroll
  for (int r = 0; r < 4; ++r) {
    auto sw = __builtin_amdgcn_permlane32_swap(f2u(a0[r]), f2u(a1[r]), false, false);
    o[r] = u2f(sw[0]) + u2f(sw[1]);
  }
  u32x2 res; res.x = pk2(o[0], o[1]); res.y = pk2(o[2], o[3]);
  return res;
}

constexpr float SCALE = 0.08838834764831845f;
constexpr float LOG2E = 1.4426950408889634f;
__device__ __forceinline__ float vmax(float a, float b) { float r; asm("v_max_f32 %0, %1, %2" : "=v"(r) : "v"(a), "v"(b)); return r; }
__device__ __forceinline__ float vmax3(float a, float b, float c) { float r; asm("v_max3_f32 %0, %1, %2, %3" : "=v"(r) : "v"(a), "v"(b), "v"(c)); return r; }
template <bool MAXOP> __device__ __forceinline__ float red_g(float v) {
  {
    auto r = __builtin_amdgcn_permlane16_swap(f2u(v), f2u(v), false, false);
    const float a = u2f(r[0]), b = u2f(r[1]);
    v = MAXOP ? vmax(a, b) : a + b;
  }
  {
    auto r = __builtin_amdgcn_permlane32_swap(f2u(v), f2u(v), false, false);
    const float a = u2f(r[0]), b = u2f(r[1]);
    v = MAXOP ? vmax(a, b) : a + b;
  }
  return v;
}

template <int ROLE>
__device__ __forceinline__ void head_loop(const Params& p, const Ctx& cx_, LAS unsigned char* L, int h, const u32x8 (&W)[2][8]) {
  const Ctx cx = fresh(cx_);
  const int w = cx.wave, l = cx.lane, g = l >> 4, n = l & 15;
  LAS unsigned char* MEMIMG = L;
  LAS unsigned char* QKIMG = L + 8192;
  LAS unsigned* XCNT = (LAS unsigned*)(L + 12288);
  LAS unsigned char* KQ = L + 16384;
  auto stage = [&](int s) {
    wait_words(cx, C_KQ + (s >> 1) * 32, 1, 64u);
    const unsigned kb = (unsigned)((w == 4 ? OFF_KX : OFF_QX) + ((size_t)s * NH + h) * 8192) + l * 16;
    LAS unsigned char* d = KQ + (s & 1) * 16384 + (w == 4 ? 0 : 8192) + l * 16;
    u32x4 pf[8];
#pragma unroll
    for (int i = 0; i < 8; ++i) pf[i] = ld_sc1(cx.ws, kb + i * 1024);
#pragma unroll
    for (int i = 0; i < 8; ++i) *(LAS u32x4*)(d + i * 1024) = pf[i];
  };
  TR_CAL(h, 0);
  if (ROLE == 2) stage(0);
  unsigned tref = 0;
  const int q4 = (l & 15) >> 2, pp = l & 3;
  LAS unsigned char* trb = QKIMG + (16 * (g >> 1) + 4 * (g & 1) + q4) * 32;
  const bool up = n >= 8;
  LAS unsigned char* qkw = QKIMG + (((2 * w + (l >> 5)) & 7) * 16 + n) * 32 + (w >> 2) * 16 + (g & 1) * 8;
  const unsigned pboff = PBASE + h * 640 + (n & 7) * 16 + (up ? 512 : g * 128);
  for (int s = 0; s < NSEG; ++s) {
    if (s > 0) timed_wait(tref, p.dh);
    __builtin_amdgcn_s_setprio(2);
    const u32x2 qk = front(p, cx, MEMIMG, W, s, tref, [] {});
    *(LAS u32x2*)qkw = qk;
    LAS unsigned char* KI = KQ + (s & 1) * 16384 + g * 512 + n * 16;
    if (ROLE == 0) {
      u32x4 Kf[2][4];
#pragma unroll
      for (int t = 0; t < 4; ++t) { Kf[0][t] = *(LAS u32x4*)(KI + t * 2048); Kf[1][t] = *(LAS u32x4*)(KI + t * 2048 + 256); }
      const unsigned paddr = ex_addr((unsigned)(s & 3) * PSLOT + pboff, cx.pol);
      BAR();
      TR(3);
      __builtin_amdgcn_s_setprio(3);
      if (s == NSEG - 1) break;
      u32x4 F[4], F2[4];
#pragma unroll
      for (int t = 0; t < 4; ++t) {
        const u32x2 lo2 = tr_read(trb + t * 1024 + (pp & 1) * 8), hi2 = tr_read(trb + t * 1024 + 256 + (pp & 1) * 8);
        F2[t] = (u32x4){lo2.x, lo2.y, hi2.x, hi2.y};
      }
#pragma unroll
      for (int t = 0; t < 4; ++t) {
        const u32x2 lo = tr_read(trb + t * 1024 + pp * 8), hi = tr_read(trb + t * 1024 + 256 + pp * 8);
        F[t] = (u32x4){lo.x, lo.y, hi.x, hi.y};
      }
      __builtin_amdgcn_sched_barrier(0);
      f32x4 s0 = {0.f, 0.f, 0.f, 0.f}, s1 = s0, sm = s0;
#pragma unroll
      for (int t = 0; t < 4; ++t) { s0 = mfma16(Kf[0][t], F2[t], s0); s1 = mfma16(Kf[1][t], F2[t], s1); }
#pragma unroll
      for (int t = 0; t < 4; ++t) sm = mfma16(F[t], F2[t], sm);
      float v[6];
#pragma unroll
      for (int r = 0; r < 4; ++r) v[r] = up ? s1[r] : s0[r];
      float mx = vmax(vmax3(v[0], v[1], v[2]), v[3]);
      v[4] = (g >= 2) ? (up ? sm[2] : sm[0]) : -INFINITY;
      v[5] = (g >= 2) ? (up ? sm[3] : sm[1]) : -INFINITY;
      mx = vmax3(mx, v[4], v[5]);
      mx = vmax(mx, u2f(dpp_ror8(f2u(mx))));
      mx = red_g<true>(mx);
      const float cs = SCALE * LOG2E, mc = -mx * cs;
#pragma unroll
      for (int i = 0; i < 6; ++i) v[i] = __builtin_amdgcn_exp2f(__builtin_fmaf(v[i], cs, mc));
      float sum = ((v[0] + v[1]) + (v[2] + v[3])) + (v[4] + v[5]);
      sum += u2f(dpp_ror8(f2u(sum)));
      sum = red_g<false>(sum);
      const float inv = __builtin_amdgcn_rcpf(sum);
      const unsigned px0 = nosent(pk2(v[0] * inv, v[1] * inv)), px1 = nosent(pk2(v[2] * inv, v[3] * inv));
      const unsigned pm = nosent(pk2(v[4] * inv, v[5] * inv));
      const unsigned o0 = dpp_ror8(px0), o1 = dpp_ror8(px1), t1 = dpp_ror8(pm);
      const unsigned d2 = __builtin_amdgcn_permlane16_swap(t1, t1, false, false)[1], d3 = __builtin_amdgcn_permlane16_swap(pm, pm, false, false)[1];
      u32x4 dat; dat.x = up ? t1 : px0; dat.y = up ? pm : px1; dat.z = up ? d2 : o0; dat.w = up ? d3 : o1;
      if (!up || g == 2) {
#pragma unroll
        for (int r = 0; r < RP; ++r) st_sc1s(cx.ex, paddr, r * 2 * PREP, dat);
      }
      TR(4);
      __builtin_amdgcn_s_setprio(0);
      if (l < 40) {
        const unsigned ra = ex_addr(PBASE + (unsigned)((s + 2) & 3) * PSLOT + h * 640 + l * 16, cx.pol);
#pragma unroll
        for (int r = 0; r < RP; ++r) st_sc1s(cx.ex, ra, r * 2 * PREP, (u32x4){SENT, SENT, SENT, SENT});
      }
    } else {
      BAR();
      TR(3);
      __builtin_amdgcn_s_setprio(0);
      if (ROLE == 2) {
        __builtin_amdgcn_s_sleep(3);
        if (s + 1 < NSEG) stage(s + 1);
      } else if (ROLE == 3) {
        __builtin_amdgcn_s_sleep(3);
        const int qt = w - 5;
        u32x4 F[4], Kf[2][4], Qf[4];
#pragma unroll
        for (int t = 0; t < 4; ++t) {
          const u32x2 lo = tr_read(trb + t * 1024 + pp * 8), hi = tr_read(trb + t * 1024 + 256 + pp * 8);
          F[t] = (u32x4){lo.x, lo.y, hi.x, hi.y};
          Kf[0][t] = *(LAS u32x4*)(KI + t * 2048); Kf[1][t] = *(LAS u32x4*)(KI + t * 2048 + 256);
          Qf[t] = *(LAS u32x4*)(KI + 8192 + t * 2048 + qt * 256);
        }
        f32x4 s0 = {0.f, 0.f, 0.f, 0.f}, s1 = s0, sm = s0;
#pragma unroll
        for (int t = 0; t < 4; ++t) { s0 = mfma16(Kf[0][t], Qf[t], s0); s1 = mfma16(Kf[1][t], Qf[t], s1); sm = mfma16(F[t], Qf[t], sm); }
        const int qi = qt * 16 + n;
        float sc[12];
#pragma unroll
        for (int r = 0; r < 4; ++r) {
          sc[r] = (4 * g + r <= qi) ? s0[r] : -INFINITY;
          sc[4 + r] = (16 + 4 * g + r <= qi) ? s1[r] : -INFINITY;
          sc[8 + r] = (g >= 2) ? sm[r] : -INFINITY;
        }
        float mx = vmax3(vmax3(sc[0], sc[1], sc[2]), vmax3(sc[3], sc[4], sc[5]), vmax3(vmax3(sc[6], sc[7], sc[8]), vmax3(sc[9], sc[10], sc[11]), sc[0]));
        mx = red_g<true>(mx);
        const float cs = SCALE * LOG2E, mc = -mx * cs;
        float sum = 0.f;
#pragma unroll
        for (int i = 0; i < 12; ++i) { sc[i] = __builtin_amdgcn_exp2f(__builtin_fmaf(sc[i], cs, mc)); sum += sc[i]; }
        sum = red_g<false>(sum);
        const float inv = __builtin_amdgcn_rcpf(sum);
        u32x4 px; px.x = pk2(sc[0] * inv, sc[1] * inv); px.y = pk2(sc[2] * inv, sc[3] * inv); px.z = pk2(sc[4] * inv, sc[5] * inv); px.w = pk2(sc[6] * inv, sc[7] * inv);
        u32x2 pm; pm.x = pk2(sc[8] * inv, sc[9] * inv); pm.y = pk2(sc[10] * inv, sc[11] * inv);
        const unsigned pb = (unsigned)(OFF_PX + ((size_t)s * NH + h) * 2560) + qi * 16;
        st_sc1(cx.ws, pb + g * 512, px);
        if (g >= 2) st_sc1_64(cx.ws, pb + 4 * 512 + (g - 2) * 8, pm);
        DRAIN();
        if (l == 0) {
          const unsigned old = __hip_atomic_fetch_add(XCNT, 1u, __ATOMIC_RELAXED, __HIP_MEMORY_SCOPE_WORKGROUP);
          if (old & 1u) st_sc1_32(cx.ex, ex_addr(FPU + h * 128, cx.pol), (unsigned)s);
        }
      }
      TR(5);
    }
  }
  TR_OUT(h);
}

__device__ __forceinline__ void chain_ready(const Ctx& cx_, int id) {
  const Ctx cx = fresh(cx_);
  if (cx.wave == 0) wait_words(cx, C_RDY, 8, 12u);
  BAR();
}

__device__ __forceinline__ void head_role(const Params& p, const Ctx& cx_, LAS unsigned char* L, int h) {
  const Ctx cx = fresh(cx_);
  const int w = cx.wave, l = cx.lane, g = l >> 4, tid = w * 64 + l;
  u32x8 W[2][8];
#pragma unroll
  for (int rd = 0; rd < 2; ++rd) {
    const u16* Wsrc = rd ? p.Wk : p.Wq;
#pragma unroll
    for (int i = 0; i < 16; ++i) {
      const int kap = i * 4 + (l >> 4), sig = l & 15;
      const int gsw = (kap & 3) | (((kap >> 3) & 1) << 2);
      const int chunk = (((sig >> 1) ^ gsw) << 1) | (sig & 1);
      const int d = 64 * w + 16 * (kap >> 4) + 8 * ((kap >> 1) & 1) + 2 * ((kap >> 2) & 3) + (kap & 1);
      GLDS(Wsrc + (size_t)d * HD + h * DH + chunk * 8, L + w * 16384 + i * 1024);
    }
    if (rd == 0) {
      if (tid == 511) st_sc1_32(cx.ex, ex_addr(FPU + h * 128, cx.pol), SENT);
#pragma unroll
      for (int k = 0; k < 2; ++k) {
        const int q = tid + k * 512;
        if (q < 4 * RP * 40) {
          const int pc = q % 40, sr = q / 40;
          st_sc1(cx.ex, ex_addr(PBASE + (unsigned)(sr >> 2) * PREP + (unsigned)(sr & 3) * PSLOT + h * 640 + pc * 16, cx.pol), (u32x4){SENT, SENT, SENT, SENT});
        }
      }
    }
    DRAIN();
    BAR();
    if (rd == 0 && tid == 0) arrive(cx, C_RDY + (h & 7) * 32);
    if ((w >> 2) == rd) {
      const int q = (l & 15) >> 2, pp = l & 3;
#pragma unroll
      for (int i = 0; i < 8; ++i) {
        LAS unsigned char* img = L + ((w + i) & 7) * 16384;
#pragma unroll
        for (int t = 0; t < 2; ++t) {
          const int et = (2 * w + t) & 7;
          u32x8 f;
#pragma unroll
          for (int jq = 0; jq < 4; ++jq) {
            const int kap = (jq >> 1) * 32 + 8 * g + (jq & 1) * 4 + q;
            const int gsw = (kap & 3) | (((kap >> 3) & 1) << 2);
            const u32x2 v = tr_read(img + kap * 256 + ((et ^ gsw) * 32) + pp * 8);
            f[jq * 2] = v.x; f[jq * 2 + 1] = v.y;
          }
          W[t][i] = f;
        }
      }
    }
    BAR();
  }
  if (tid == 0) *(LAS unsigned*)(L + 12288) = 0u;
  TRG(h, 1);
  chain_ready(cx, h);
  TRG(h, 2);
  if (w == 0) head_loop<0>(p, cx, L, h, W);
  else if (w < 4) head_loop<1>(p, cx, L, h, W);
  else if (w == 4 || w == 7) head_loop<2>(p, cx, L, h, W);
  else head_loop<3>(p, cx, L, h, W);
}

__device__ __forceinline__ void wo_issue(const Params& p, const Ctx& cx_, LAS unsigned char* L, int pid) {
  const Ctx cx = fresh(cx_);
  const int w = cx.wave, l = cx.lane, h = pid & 31, qr = pid >> 5;
#pragma unroll
  for (int i = 0; i < 4; ++i) {
    const int r = (w * 4 + i) * 4 + (l >> 4);
    GLDS(p.Wv + (size_t)(128 * qr + r) * HD + h * DH + ((l & 15) ^ (r & 15)) * 8, L + 131072 + (w * 4 + i) * 1024);
  }
#pragma unroll
  for (int i = 0; i < 16; ++i) {
    const int e = w * 16 + i;
    GLDS(p.Wo + ((size_t)h * DH + e) * DM + ((((l >> 2) ^ (i & 3)) << 2) + (l & 3)) * 8, L + e * 1024);
  }
}
__device__ __forceinline__ void wvo_part(const Params& p, const Ctx& cx_, LAS unsigned char* L, int pid, u32x4 (&B)[2][8]) {
  const Ctx cx = fresh(cx_);
  const int w = cx.wave, l = cx.lane, hi = l >> 5, h = pid & 31, qr = pid >> 5;
  {
    const int blk = (l >> 4) & 1, q = (l & 15) >> 2, pp = l & 3;
#pragma unroll
    for (int nt = 0; nt < 2; ++nt) {
      LAS unsigned char* b0 = L + (4 * hi + q) * 1024 + ((2 * w + nt) ^ q) * 64 + blk * 32 + pp * 8;
#pragma unroll
      for (int u = 0; u < 8; ++u) {
        const u32x2 lo = tr_read(b0 + u * 16384), hi2 = tr_read(b0 + u * 16384 + 8192);
        B[nt][u] = (u32x4){lo.x, lo.y, hi2.x, hi2.y};
      }
    }
  }
#pragma unroll
  for (int i = 0; i < 4; ++i) {
    const int dt = qr * 4 + i;
    u32x4 A[8];
    LAS unsigned char* a0 = L + 131072 + (32 * i + (l & 31)) * 256 + hi * 8;
#pragma unroll
    for (int u = 0; u < 8; ++u) {
      const u32x2 lo = *(LAS u32x2*)(a0 + (((2 * u) ^ (l & 15)) << 4)), h2 = *(LAS u32x2*)(a0 + (((2 * u + 1) ^ (l & 15)) << 4));
      A[u] = (u32x4){lo.x, lo.y, h2.x, h2.y};
    }
#pragma unroll
    for (int nt = 0; nt < 2; ++nt) {
      f32x16 acc = {0.f};
#pragma unroll
      for (int u = 0; u < 8; ++u) acc = mfma32(A[u], B[nt][u], acc);
      const int col = 64 * w + 32 * nt + (l & 31);
#pragma unroll
      for (int b16 = 0; b16 < 2; ++b16) {
        const int d16 = 2 * dt + b16, T = d16 >> 2, gA = d16 & 3;
        const int lane2 = (2 * (gA & 1) + hi) * 16 + (h & 1) * 8 + (col & 7);
        u32x4 o;
        o.x = pk2(acc[8 * b16 + 0], acc[8 * b16 + 1]); o.y = pk2(acc[8 * b16 + 4], acc[8 * b16 + 5]);
        o.z = pk2(acc[8 * b16 + 2], acc[8 * b16 + 3]); o.w = pk2(acc[8 * b16 + 6], acc[8 * b16 + 7]);
        st_sc1(cx.ws, (unsigned)(OFF_WVO + ((((size_t)(col >> 3) * 16 + (h >> 1)) * 8 + T) * 2048) + (gA >> 1) * 1024 + lane2 * 16), o);
      }
    }
  }
  TRG(pid < 32 ? 128 + pid * 4 + 3 : (pid < 64 ? 64 + pid : pid - 32), 5);
  DRAIN();
  BAR();
  if (w == 0 && l == 0) arrive(cx, C_WVO + (pid & 7) * 32);
}

template <int ROLE>
__device__ __forceinline__ void col_loop(const Params& p, const Ctx& cx_, LAS unsigned char* L, int c, const u32x8 (&W)[2][8]) {
  const Ctx cx = fresh(cx_);
  const int w = cx.wave, l = cx.lane, g = l >> 4, n = l & 15, tid = w * 64 + l;
  LAS unsigned char* MEMIMG = L;
  LAS unsigned* UCNT = (LAS unsigned*)(L + 8192);
  LAS unsigned char* RED = L + 9216;
  LAS unsigned char* UIMG = L + 12288;
  const int ht = tid - 256;
  unsigned uoff[4];
#pragma unroll
  for (int i = 0; i < 4; ++i) { const int ch4 = (ht >> 3) + 32 * i; uoff[i] = (unsigned)((((ch4 >> 2) * 5 + (ch4 & 3)) * 8 + (ht & 7)) * 16); }
  u32x4 ur[4];
  auto uload = [&](int s) {
    const unsigned base = (unsigned)(OFF_U + ((size_t)s * NCOL + c) * U_SC);
#pragma unroll
    for (int i = 0; i < 4; ++i) ur[i] = ld_sc1(cx.ws, base + uoff[i]);
  };
  auto ustore = [&](int s) {
    LAS unsigned char* UI = UIMG + (s & 1) * 20480;
#pragma unroll
    for (int i = 0; i < 4; ++i) *(LAS u32x4*)(UI + uoff[i]) = ur[i];
  };
  TR_CAL(32 + c, 0);
  if (ROLE == 2) { wait_words(cx, C_U, 2, 32u); uload(0); ustore(0); }
  unsigned tb1 = 0, tpub = 0;
  const unsigned umo2 = (unsigned)((((ht >> 3) * 5 + 4) * 8 + (ht & 7)) * 16);
  const unsigned umo = (unsigned)((((2 * (2 * w + (l >> 5)) + (n >> 3)) * 5 + 4) * 8 + (n & 7)) * 16 + (g & 1) * 8);
  for (int s = 0; s < NSEG; ++s) {
    LAS unsigned char* UI = UIMG + (s & 1) * 20480;
    u32x4 A0[5], A1[5], A2[5];
    u32x4 umprev = {0, 0, 0, 0};
    if (ROLE == 2 && s > 0) umprev = *(LAS u32x4*)(UIMG + ((s - 1) & 1) * 20480 + umo2);
    if (s > 0) timed_wait(tpub, p.dm);
    const u32x2 um = front(p, cx, MEMIMG, W, s, tb1, [&] {
      if (ROLE == 2) {
        if (s + 1 < NSEG - 1) uload(s + 1);
        if (s > 0) st_sc1(cx.ws, (unsigned)(OFF_U + ((size_t)(s - 1) * NCOL + c) * U_SC) + umo2, umprev);
      }
    });
    *(LAS u32x2*)(UI + umo) = um;
    BAR();
    TR(3);
    if (ROLE == 2) {
      if (s == NSEG - 1) st_sc1(cx.ws, (unsigned)(OFF_U + ((size_t)s * NCOL + c) * U_SC) + umo2, *(LAS u32x4*)(UI + umo2));
      DRAIN();
      if (s == NSEG - 1) {
        if (l == 0) {
          const unsigned old = __hip_atomic_fetch_add(UCNT, 1u, __ATOMIC_RELAXED, __HIP_MEMORY_SCOPE_WORKGROUP);
          if (old == 3u) st_sc1_32(cx.ex, ex_addr(FUMU + c * 128, cx.pol), (unsigned)s);
        }
        break;
      }
      if (s + 1 < NSEG - 1) ustore(s + 1);
      if (s + 2 < NSEG - 1) wait_words(cx, C_U + (s + 2) * 32, 1, 32u);
      TR(5);
    } else {
      if (s == NSEG - 1) break;
      u32x4 B[5];
      unsigned aoff[5];
#pragma unroll
      for (int i = 0; i < 5; ++i) {
        const int ch = (n >> 3) * 80 + 4 * (w * 5 + i) + g;
        B[i] = *(LAS u32x4*)(UI + (ch * 8 + (n & 7)) * 16);
        aoff[i] = ex_addr(PBASE + (unsigned)(s & 3) * PSLOT + ch * 128 + (n & 7) * 16, cx.pol);
      }
      timed_wait(tb1, p.dp);
      __builtin_amdgcn_s_setprio(3);
      TR(4);
      f32x4 acc; unsigned t_, cnt, sr;
      asm volatile(
        "s_mov_b32 %[sr], 0\n\t"
        "buffer_load_dwordx4 v[196:199], %[o0], %[rs], %[sr] offen sc1\n\t"
        "buffer_load_dwordx4 v[200:203], %[o1], %[rs], %[sr] offen sc1\n\t"
        "buffer_load_dwordx4 v[204:207], %[o2], %[rs], %[sr] offen sc1\n\t"
        "buffer_load_dwordx4 v[208:211], %[o3], %[rs], %[sr] offen sc1\n\t"
        "buffer_load_dwordx4 v[212:215], %[o4], %[rs], %[sr] offen sc1\n\t"
        "s_add_u32 %[sr], %[sr], 196608\n\t"
        "s_cmp_eq_u32 %[sr], 589824\n\t"
        "s_cselect_b32 %[sr], 0, %[sr]\n\t"
        "s_mov_b32 %[cnt], 0\n\t"
        "L%=_1:\n\t"
        "s_waitcnt vmcnt(4)\n\t"
        "v_mfma_f32_16x16x32_bf16 %[acc], v[196:199], %[b0], 0\n\t"
        "v_max3_u32 %[t], v196, v197, v198\n\t"
        "v_max_u32 %[t], %[t], v199\n\t"
        "s_waitcnt vmcnt(3)\n\t"
        "v_mfma_f32_16x16x32_bf16 %[acc], v[200:203], %[b1], %[acc]\n\t"
        "v_max3_u32 %[t], %[t], v200, v201\n\t"
        "v_max3_u32 %[t], %[t], v202, v203\n\t"
        "s_waitcnt vmcnt(2)\n\t"
        "v_mfma_f32_16x16x32_bf16 %[acc], v[204:207], %[b2], %[acc]\n\t"
        "v_max3_u32 %[t], %[t], v204, v205\n\t"
        "v_max3_u32 %[t], %[t], v206, v207\n\t"
        "s_waitcnt vmcnt(1)\n\t"
        "v_mfma_f32_16x16x32_bf16 %[acc], v[208:211], %[b3], %[acc]\n\t"
        "v_max3_u32 %[t], %[t], v208, v209\n\t"
        "v_max3_u32 %[t], %[t], v210, v211\n\t"
        "s_waitcnt vmcnt(0)\n\t"
        "v_mfma_f32_16x16x32_bf16 %[acc], v[212:215], %[b4], %[acc]\n\t"
        "v_max3_u32 %[t], %[t], v212, v213\n\t"
        "v_max3_u32 %[t], %[t], v214, v215\n\t"
        "v_cmp_eq_u32 vcc, -1, %[t]\n\t"
        "s_cbranch_vccz L%=_20\n\t"
        "buffer_load_dwordx4 v[196:199], %[o0], %[rs], %[sr] offen sc1\n\t"
        "buffer_load_dwordx4 v[200:203], %[o1], %[rs], %[sr] offen sc1\n\t"
        "buffer_load_dwordx4 v[204:207], %[o2], %[rs], %[sr] offen sc1\n\t"
        "buffer_load_dwordx4 v[208:211], %[o3], %[rs], %[sr] offen sc1\n\t"
        "buffer_load_dwordx4 v[212:215], %[o4], %[rs], %[sr] offen sc1\n\t"
        "s_add_u32 %[sr], %[sr], 196608\n\t"
        "s_cmp_eq_u32 %[sr], 589824\n\t"
        "s_cselect_b32 %[sr], 0, %[sr]\n\t"
        "s_add_u32 %[cnt], %[cnt], 1\n\t"
        DP_NOPS2 "\n\t"
        "s_cmp_lt_u32 %[cnt], %[cap]\n\t"
        "s_cbranch_scc1 L%=_1\n\t"
        "s_waitcnt vmcnt(0)\n\t"
        "L%=_20:\n\t"
        "s_nop 7\n\t"
        : "=&{v[196:199]}"(A0[0]), "=&{v[200:203]}"(A0[1]), "=&{v[204:207]}"(A0[2]), "=&{v[208:211]}"(A0[3]), "=&{v[212:215]}"(A0[4]), "=&{v[216:219]}"(A1[0]), "=&{v[220:223]}"(A1[1]), "=&{v[224:227]}"(A1[2]), "=&{v[228:231]}"(A1[3]), "=&{v[232:235]}"(A1[4]), "=&{v[236:239]}"(A2[0]), "=&{v[240:243]}"(A2[1]), "=&{v[244:247]}"(A2[2]), "=&{v[248:251]}"(A2[3]), "=&{v[252:255]}"(A2[4]), [acc] "=&v"(acc), [t] "=&v"(t_), [cnt] "=&s"(cnt), [sr] "=&s"(sr)
        : [o0] "v"(aoff[0]), [o1] "v"(aoff[1]), [o2] "v"(aoff[2]), [o3] "v"(aoff[3]), [o4] "v"(aoff[4]), [b0] "v"(B[0]), [b1] "v"(B[1]), [b2] "v"(B[2]), [b3] "v"(B[3]), [b4] "v"(B[4]),
          [rs] "s"(cx.exs), [cap] "s"(SPIN_CAP)
        : "vcc", "scc", "memory");
      if (cnt >= SPIN_CAP) __hip_atomic_store(cx.ctrl + C_TMO, 1u, RLX_AGENT);
      TR(5);
      if ((n >> 3) == (g >> 1)) *(LAS f32x4*)(RED + (((w * 2 + (g >> 1)) * 8 + (n & 7)) * 8 + 4 * (g & 1)) * 4) = acc;
    }
    BAR();
    TR(6);
    tpub = (unsigned)__builtin_amdgcn_s_memtime();
    if (ROLE == 2 && s > 0 && tid == 256) st_sc1_32(cx.ex, ex_addr(FUMU + c * 128, cx.pol), (unsigned)(s - 1));
    if (ROLE == 0) {
      const int m = l >> 3, c8 = l & 7;
      float x[8];
#pragma unroll
      for (int sl = 0; sl < 8; ++sl) x[sl] = *(LAS float*)(RED + ((sl * 8 + c8) * 8 + m) * 4);
      const float v = ((x[0] + x[1]) + (x[2] + x[3])) + ((x[4] + x[5]) + (x[6] + x[7]));
      const float o = u2f(__builtin_amdgcn_update_dpp(0, f2u(v), 0xB1, 0xf, 0xf, true));
      const unsigned d = nosent(pk2(v, o));
      u32x4 dat; dat.x = d;
      dat.y = (unsigned)__builtin_amdgcn_update_dpp(0, (int)d, 0xAA, 0xf, 0xf, true);
      dat.z = (unsigned)__builtin_amdgcn_update_dpp(0, (int)d, 0x104, 0xf, 0xf, true);
      dat.w = (unsigned)__builtin_amdgcn_update_dpp(0, (int)dat.y, 0x104, 0xf, 0xf, true);
      if (c8 == 0) {
        const unsigned ma = ex_addr((unsigned)((s + 1) & 3) * 8192u + c * 128 + m * 16, cx.pol);
#pragma unroll
        for (int r = 0; r < RM; ++r) st_sc1s(cx.ex, ma, r * 2 * MREP, dat);
      }
      TR(7);
      if (l < 8 * RM) st_sc1(cx.ex, ex_addr((unsigned)((s + 3) & 3) * 8192u + c * 128 + (l & 7) * 16, cx.pol) + (l >> 3) * 2 * MREP, (u32x4){SENT, SENT, SENT, SENT});
    }
    __builtin_amdgcn_s_setprio(0);
    if (ROLE == 0) asm volatile("s_waitcnt vmcnt(5)" : "+{v[196:199]}"(A0[0]), "+{v[200:203]}"(A0[1]), "+{v[204:207]}"(A0[2]), "+{v[208:211]}"(A0[3]), "+{v[212:215]}"(A0[4]), "+{v[216:219]}"(A1[0]), "+{v[220:223]}"(A1[1]), "+{v[224:227]}"(A1[2]), "+{v[228:231]}"(A1[3]), "+{v[232:235]}"(A1[4]), "+{v[236:239]}"(A2[0]), "+{v[240:243]}"(A2[1]), "+{v[244:247]}"(A2[2]), "+{v[248:251]}"(A2[3]), "+{v[252:255]}"(A2[4]) :: "memory");
    if (ROLE == 1) asm volatile("s_waitcnt vmcnt(0)" : "+{v[196:199]}"(A0[0]), "+{v[200:203]}"(A0[1]), "+{v[204:207]}"(A0[2]), "+{v[208:211]}"(A0[3]), "+{v[212:215]}"(A0[4]), "+{v[216:219]}"(A1[0]), "+{v[220:223]}"(A1[1]), "+{v[224:227]}"(A1[2]), "+{v[228:231]}"(A1[3]), "+{v[232:235]}"(A1[4]), "+{v[236:239]}"(A2[0]), "+{v[240:243]}"(A2[1]), "+{v[244:247]}"(A2[2]), "+{v[248:251]}"(A2[3]), "+{v[252:255]}"(A2[4]) :: "memory");
  }
  TR_OUT(32 + c);
}

__device__ __forceinline__ void col_role(const Params& p, const Ctx& cx_, LAS unsigned char* L, int c) {
  const int w = cx_.wave;
  if (w < RM / 2) {
    const Ctx cx = fresh(cx_);
    const int tid = w * 64 + cx.lane;
    const int pc = tid & 7, sr = tid >> 3;
    if (tid == 0) st_sc1_32(cx.ex, ex_addr(FUMU + c * 128, cx.pol), SENT);
    st_sc1(cx.ex, ex_addr((unsigned)(sr >> 2) * MREP + (unsigned)(sr & 3) * 8192u + c * 128 + pc * 16, cx.pol), (u32x4){SENT, SENT, SENT, SENT});
  }
  {
    u32x4 B[2][8];
    wo_issue(p, cx_, L, 64 + c);
    DRAIN();
    BAR();
    { const Ctx cx = fresh(cx_); TRG(32 + c, 4); if (w == 0 && cx.lane == 0) arrive(cx, C_RDY + (c & 7) * 32); }
    wvo_part(p, cx_, L, 64 + c, B);
  }
  const Ctx cx = fresh(cx_);
  const int l = cx.lane, tid = w * 64 + l;
  TRG(32 + c, 1);
  if (w == 0) wait_words(cx, C_WVO, 8, 16u);
  BAR();
  TRG(32 + c, 2);
  u32x8 W[2][8];
#pragma unroll
  for (int t = 0; t < 2; ++t)
#pragma unroll
    for (int i = 0; i < 8; ++i) {
      const unsigned off = (unsigned)(OFF_WVO + (((size_t)c * 16 + 2 * w + t) * 8 + ((w + i) & 7)) * 2048) + l * 16;
      const u32x4 a = ld_sc1(cx.ws, off), b = ld_sc1(cx.ws, off + 1024);
      W[t][i] = (u32x8){a.x, a.y, a.z, a.w, b.x, b.y, b.z, b.w};
    }
  if (tid == 0) *(LAS unsigned*)(L + 8192) = 0u;
  chain_ready(cx, c);
  TRG(32 + c, 3);
  if (w == 0) col_loop<0>(p, cx, L, c, W);
  else if (w < 4) col_loop<1>(p, cx, L, c, W);
  else col_loop<2>(p, cx, L, c, W);
}

template <int CTRL, int RMASK> __device__ __forceinline__ unsigned dppu(unsigned old, unsigned x) { return (unsigned)__builtin_amdgcn_update_dpp((int)old, (int)x, CTRL, RMASK, 0xf, false); }
__device__ __forceinline__ float wave_sum(float v) {
  v += u2f(dppu<0x111, 0xf>(0u, f2u(v)));
  v += u2f(dppu<0x112, 0xf>(0u, f2u(v)));
  v += u2f(dppu<0x114, 0xf>(0u, f2u(v)));
  v += u2f(dppu<0x118, 0xf>(0u, f2u(v)));
  v += u2f(dppu<0x142, 0xa>(0u, f2u(v)));
  v += u2f(dppu<0x143, 0xc>(0u, f2u(v)));
  return u2f((unsigned)__builtin_amdgcn_readlane((int)f2u(v), 63));
}
__device__ __forceinline__ unsigned wave_umax(unsigned v) {
  v = max(v, dppu<0x111, 0xf>(0u, v));
  v = max(v, dppu<0x112, 0xf>(0u, v));
  v = max(v, dppu<0x114, 0xf>(0u, v));
  v = max(v, dppu<0x118, 0xf>(0u, v));
  v = max(v, dppu<0x142, 0xa>(0u, v));
  v = max(v, dppu<0x143, 0xc>(0u, v));
  return (unsigned)__builtin_amdgcn_readlane((int)v, 63);
}
template <int NR>
__device__ __forceinline__ void ln_part(const Params& p, const Ctx& cx_, int row0, int id) {
  const Ctx cx = fresh(cx_);
  const int w = cx.wave, l = cx.lane;
  const u32x4 wr = *(const GAS u32x4*)(p.w_r + l * 8);
  u32x4 xv[NR];
#pragma unroll
  for (int k = 0; k < NR; ++k) xv[k] = *(const GAS u32x4*)(p.x + (size_t)(row0 + NR * w + k) * DM + l * 8);
#pragma unroll
  for (int k = 0; k < NR; ++k) {
    const int row = row0 + NR * w + k;
    float v[8];
#pragma unroll
    for (int i = 0; i < 4; ++i) { v[2 * i] = bflo(xv[k][i]); v[2 * i + 1] = bfhi(xv[k][i]); }
    const float mean = wave_sum(((v[0] + v[1]) + (v[2] + v[3])) + ((v[4] + v[5]) + (v[6] + v[7]))) * (1.f / DM);
    float q = 0.f;
#pragma unroll
    for (int i = 0; i < 8; ++i) { const float d = v[i] - mean; q += d * d; }
    const float mb = bfr(mean), vb = bfr(wave_sum(q) * (1.f / DM));
    const float den = bfr(sqrtf(bfr(vb + 1e-5f)));
    float r = 0.f;
#pragma unroll
    for (int i = 0; i < 8; ++i) v[i] = bfr(bfr(v[i] - mb) / den);
    u32x4 o; o.x = pk2(v[0], v[1]); o.y = pk2(v[2], v[3]); o.z = pk2(v[4], v[5]); o.w = pk2(v[6], v[7]);
    st_sc1(cx.ws, (unsigned)(OFF_XN + (size_t)row * 1024) + l * 16, o);
#pragma unroll
    for (int i = 0; i < 4; ++i) r += v[2 * i] * bflo(wr[i]) + v[2 * i + 1] * bfhi(wr[i]);
    r = bfr(wave_sum(r));
    if (l == 0) st_sc1(cx.ws, (unsigned)(OFF_STAT + (size_t)row * 16), (u32x4){f2u(mb), f2u(den), f2u(r), 0u});
  }
  DRAIN();
  BAR();
  if (w == 0 && l == 0) arrive(cx, C_XN + (id & 7) * 32);
}
__device__ __forceinline__ void stage_w(const u16* Wsrc, int h, const Ctx& cx, LAS unsigned char* L) {
  const int w = cx.wave, l = cx.lane;
#pragma unroll
  for (int i = 0; i < 16; ++i) {
    const int row = w * 64 + i * 4 + (l >> 4), sig = l & 15;
    const int chunk = (((sig >> 1) ^ ((row & 3) << 1)) << 1) | (sig & 1);
    GLDS(Wsrc + (size_t)row * HD + h * DH + chunk * 8, L + w * 16384 + i * 1024);
  }
}
__device__ __forceinline__ void read_wt(const Ctx& cx, LAS unsigned char* L, int nt, u32x4 (&A)[32]) {
  const int l = cx.lane, hi = l >> 5, blk = (l >> 4) & 1, q = (l & 15) >> 2, pp = l & 3;
  LAS unsigned char* b0 = L + (8 * hi + q) * 256 + (((2 * nt + blk) ^ (q << 1)) * 32) + pp * 8;
#pragma unroll
  for (int u = 0; u < 32; ++u) {
    const u32x2 lo = tr_read(b0 + u * 4096), h2 = tr_read(b0 + u * 4096 + 1024);
    A[u] = (u32x4){lo.x, lo.y, h2.x, h2.y};
  }
}
constexpr int XSTR = 1040;
__device__ __forceinline__ void xn_ready(const Ctx& cx_) {
  const Ctx cx = fresh(cx_);
  if (cx.wave == 0) {
    wait_words(cx, C_XN, 8, 12u);
    __builtin_amdgcn_fence(__ATOMIC_ACQUIRE, "agent");
    DRAIN();
  }
  BAR();
}
__device__ __forceinline__ void proj_tile(const Ctx& cx, LAS unsigned char* xt, const u32x4 (&A)[32], unsigned dst) {
  const int l = cx.lane, hi = l >> 5;
  LAS unsigned char* b = xt + (l & 31) * XSTR + hi * 16;
  f32x16 acc = {0.f};
#pragma unroll
  for (int u = 0; u < 32; ++u) acc = mfma32(A[u], *(LAS u32x4*)(b + u * 32), acc);
#pragma unroll
  for (int q = 0; q < 2; ++q) {
    u32x4 o;
    o.x = pk2(acc[8 * q + 0], acc[8 * q + 1]); o.y = pk2(acc[8 * q + 2], acc[8 * q + 3]);
    o.z = pk2(acc[8 * q + 4], acc[8 * q + 5]); o.w = pk2(acc[8 * q + 6], acc[8 * q + 7]);
    st_sc1(cx.ws, dst + (2 * q + hi) * 512 + (l & 31) * 16, o);
  }
}
__device__ __forceinline__ void mod_part(const Params& p, const Ctx& cx_, LAS unsigned char* L, int t32);
__device__ __forceinline__ void proj_role(const Params& p, const Ctx& cx, LAS unsigned char* L, int h, int kind, int j) {
  const int w = cx.wave, l = cx.lane;
  {
    u32x4 A[32];
    if (kind == 2) ln_part<2>(p, cx, 64 * h + 48, 3 * h + 2);
    else ln_part<3>(p, cx, 64 * h + 24 * kind, 3 * h + kind);
    TRG(128 + j, 2);
    stage_w(kind == 0 ? p.Wq : (kind == 1 ? p.Wk : p.Wv), h, cx, L);
    xn_ready(cx);
    DRAIN();
    BAR();
    read_wt(cx, L, w & 3, A);
    BAR();
    const u16* xn = (const u16*)(p.ws + OFF_XN);
    const size_t obase = kind == 0 ? OFF_QX : (kind == 1 ? OFF_KX : OFF_VX);
    auto dma = [&](int T, int buf) {
#pragma unroll
      for (int i = 0; i < 8; ++i) GLDS(xn + (size_t)(64 * T + 8 * w + i) * DM + l * 8, L + buf * (64 * XSTR) + (8 * w + i) * XSTR);
    };
    dma(0, 0);
    DRAIN();
    BAR();
#pragma unroll 1
    for (int T = 0; T < 32; ++T) {
      if (T + 1 < 32) dma(T + 1, (T + 1) & 1);
      const int s = 2 * T + (w >> 2);
      proj_tile(cx, L + (T & 1) * (64 * XSTR) + (w >> 2) * (32 * XSTR), A, (unsigned)(obase + ((size_t)s * NH + h) * 8192) + (w & 3) * 2048);
      DRAIN();
      BAR();
      if (w == 0 && l == 0) {
        if (kind == 2) __hip_atomic_store(cx.ctrl + C_V + h * 32, (unsigned)T, RLX_AGENT);
        else arrive(cx, C_KQ + T * 32);
      }
      if (T == 0) TRG(128 + j, 3);
    }
  }
  if (kind == 2) mod_part(p, cx, L, h);
}
__device__ __forceinline__ void u_role(const Params& p, const Ctx& cx_, LAS unsigned char* L, int h, int j) {
  u32x4 B[2][8];
  wo_issue(p, cx_, L, h);
  DRAIN();
  BAR();
  { const Ctx cx = fresh(cx_); TRG(128 + j, 2); }
  wvo_part(p, cx_, L, h, B);
  const Ctx cx = fresh(cx_);
  const int w = cx.wave, l = cx.lane, hi = l >> 5;
#pragma unroll 1
  for (int T = 0; T < 32; ++T) {
    TRU(0);
    wait_words(cx, C_V + h * 32, 1, (unsigned)(T + 1));
    TRU(1);
    u32x4 A[2][8];
#pragma unroll
    for (int k = 0; k < 2; ++k)
#pragma unroll
      for (int u = 0; u < 8; ++u) A[k][u] = ld_sc1(cx.ws, (unsigned)(OFF_VX + ((size_t)(2 * T + k) * NH + h) * 8192) + (2 * u + hi) * 512 + (l & 31) * 16);
#pragma unroll
    for (int k = 0; k < 2; ++k) {
#pragma unroll
      for (int nt = 0; nt < 2; ++nt) {
        f32x16 acc = {0.f};
#pragma unroll
        for (int u = 0; u < 8; ++u) acc = mfma32(A[k][u], B[nt][u], acc);
        const int col = 64 * w + 32 * nt + (l & 31);
        const unsigned base = (unsigned)(OFF_U + ((size_t)(2 * T + k) * NCOL + (col >> 3)) * U_SC) + ((h * 5 + hi) * 8 + (col & 7)) * 16;
        u32x4 o;
        o.x = pk2(acc[0], acc[1]); o.y = pk2(acc[2], acc[3]); o.z = pk2(acc[8], acc[9]); o.w = pk2(acc[10], acc[11]);
        st_sc1(cx.ws, base, o);
        o.x = pk2(acc[4], acc[5]); o.y = pk2(acc[6], acc[7]); o.z = pk2(acc[12], acc[13]); o.w = pk2(acc[14], acc[15]);
        st_sc1(cx.ws, base + 256, o);
      }
    }
    TRU(2);
    DRAIN();
    BAR();
    TRU(3);
    if (w == 0 && l < 2) arrive(cx, C_U + (2 * T + l) * 32);
    if (T == 0) TRG(128 + j, 3);
  }
}

__device__ __forceinline__ void topk_part(const Ctx& cx, LAS int* IDX) {
  const int w = cx.wave, l = cx.lane, tid = w * 64 + l;
  LAS unsigned* CAND = (LAS unsigned*)(IDX + 16);
  unsigned key[4];
#pragma unroll
  for (int i = 0; i < 4; ++i) {
    const int row = tid + 512 * i;
    const unsigned rb = __builtin_amdgcn_raw_buffer_load_b32(cx.ws, (unsigned)(OFF_STAT + (size_t)row * 16 + 8), 0, AUX_SC1);
    const unsigned mono = (rb & 0x80000000u) ? ~rb : (rb | 0x80000000u);
    key[i] = (mono & 0xFFFF0000u) | (unsigned)(0xFFFF - row);
  }
  unsigned mine = 0;
#pragma unroll
  for (int c = 0; c < TOPC; ++c) {
    const unsigned b = wave_umax(max(max(key[0], key[1]), max(key[2], key[3])));
#pragma unroll
    for (int i = 0; i < 4; ++i) if (key[i] == b) key[i] = 0u;
    if (l == c) mine = b;
  }
  if (l < 8) CAND[w * 8 + l] = mine;
  BAR();
  unsigned k2 = CAND[l];
#pragma unroll
  for (int c = 0; c < TOPC; ++c) {
    const unsigned b = wave_umax(k2);
    if (k2 == b) k2 = 0u;
    if (l == c) mine = b;
  }
  if (w == 0 && l < 8) {
    const unsigned mono = mine & 0xFFFF0000u;
    const float r = u2f((mono & 0x80000000u) ? (mono & 0x7fffffffu) : (~mono & 0xFFFF0000u));
    IDX[l] = 0xFFFF - (int)(mine & 0xFFFFu);
    IDX[8 + l] = (int)f2u(bfr(1.f / (1.f + __expf(-r))));
  }
  BAR();
}

__device__ __forceinline__ void mod_part(const Params& p, const Ctx& cx_, LAS unsigned char* L, int t32) {
  const Ctx cx = fresh(cx_);
  const int w = cx.wave, l = cx.lane, tid = w * 64 + l, g = l >> 4, n = l & 15;
  LAS int* IDX = (LAS int*)(L + 140 * 1024);
  LAS float* UX = (LAS float*)(L + 141 * 1024);
  if (t32 < 28) {
    const u16* wsrc = (w < 4 ? p.w_gate : p.w_up) + 64 * t32 + (l & 7) * 8;
#pragma unroll
    for (int i = 0; i < 16; ++i) {
      const int blk = (w & 3) * 16 + i;
      GLDS(wsrc + (size_t)(8 * blk + (l >> 3)) * FF, L + (w >> 2) * 65536 + blk * 1024);
    }
  }
  topk_part(cx, IDX);
  DRAIN();
  BAR();
  const int q4 = n >> 2, pp = l & 3;
  if (t32 < 28) {
    const unsigned xb = (unsigned)(OFF_XN + (size_t)IDX[l & 7] * 1024) + g * 16;
    u32x4 A[16];
#pragma unroll
    for (int u = 0; u < 16; ++u) A[u] = ld_sc1(cx.ws, xb + u * 64);
    LAS unsigned char* b0 = L + (w >> 2) * 65536 + (8 * g + q4) * 128 + (w & 3) * 32 + pp * 8;
    f32x4 acc = {0.f, 0.f, 0.f, 0.f};
#pragma unroll
    for (int u = 0; u < 16; ++u) {
      const u32x2 lo = tr_read(b0 + u * 4096), h2 = tr_read(b0 + u * 4096 + 512);
      acc = mfma16(A[u], (u32x4){lo.x, lo.y, h2.x, h2.y}, acc);
    }
    if (w >= 4 && g < 2) {
#pragma unroll
      for (int r = 0; r < 4; ++r) UX[(4 * g + r) * 64 + (w & 3) * 16 + n] = bfr(acc[r]);
    }
    BAR();
    LAS u16* GT = (LAS u16*)(L + 144 * 1024);
    if (w < 4 && g < 2) {
#pragma unroll
      for (int r = 0; r < 4; ++r) {
        const float gs = bfr(acc[r]);
        const float sg = bfr(gs / (1.f + __expf(-gs)));
        GT[(4 * g + r) * 64 + w * 16 + n] = f2bf(sg * UX[(4 * g + r) * 64 + w * 16 + n]);
      }
    }
    BAR();
    if (w == 0) {
      st_sc1(cx.ws, (unsigned)(OFF_GU + (size_t)(l >> 3) * (FF * 2) + t32 * 128 + (l & 7) * 16), *(LAS u32x4*)((LAS unsigned char*)GT + l * 16));
      DRAIN();
      if (l == 0) arrive(cx, C_GU);
    }
  }
  {
    const u16* wsrc = p.w_down + 16 * t32 + (l & 1) * 8;
#pragma unroll
    for (int i = 0; i < 7; ++i) {
      const int blk = w * 7 + i;
      GLDS(wsrc + (size_t)(32 * blk + (l >> 1)) * DM, L + blk * 1024);
    }
    if (w == 0) wait_words(cx, C_GU, 1, 28u);
    DRAIN();
    BAR();
    const unsigned gb = (unsigned)(OFF_GU + (size_t)(l & 7) * (FF * 2)) + (7 * w) * 64 + g * 16;
    u32x4 A[7];
#pragma unroll
    for (int u = 0; u < 7; ++u) A[u] = ld_sc1(cx.ws, gb + u * 64);
    LAS unsigned char* b0 = L + (7 * w) * 1024 + (8 * g + q4) * 32 + pp * 8;
    f32x4 acc = {0.f, 0.f, 0.f, 0.f};
#pragma unroll
    for (int u = 0; u < 7; ++u) {
      const u32x2 lo = tr_read(b0 + u * 1024), h2 = tr_read(b0 + u * 1024 + 128);
      acc = mfma16(A[u], (u32x4){lo.x, lo.y, h2.x, h2.y}, acc);
    }
    LAS float* PR = (LAS float*)(L + 65536);
    if (g < 2) {
#pragma unroll
      for (int r = 0; r < 4; ++r) PR[(w * 8 + 4 * g + r) * 16 + n] = acc[r];
    }
    BAR();
    if (w < 2) {
      const int r = tid >> 4;
      float y = 0.f;
#pragma unroll
      for (int k = 0; k < 8; ++k) y += PR[(k * 8 + r) * 16 + n];
      st_sc1_32(cx.ws, (unsigned)(OFF_Y + (size_t)(r * DM + 16 * t32 + n) * 4), f2u(bfr(bfr(y) * u2f((unsigned)IDX[8 + r]))));
      DRAIN();
    }
    BAR();
    if (tid == 0) arrive(cx, C_Y);
  }
}

__device__ __forceinline__ void to_role(const Params& p, const Ctx& cx_, LAS unsigned char* L, int x, int f) {
  const int t32 = x * 8 + f;
  {
    u32x4 B[2][8];
    wo_issue(p, cx_, L, 32 + t32);
    DRAIN();
    BAR();
    wvo_part(p, cx_, L, 32 + t32, B);
  }
  const Ctx cx = fresh(cx_);
  const int w = cx.wave, l = cx.lane, tid = w * 64 + l, hi = l >> 5;
  TRG(96 + t32, 2);
  LAS int* IDX = (LAS int*)(L + 140 * 1024);
  LAS f32x4* SELT = (LAS f32x4*)(L + 141 * 1024);
  if (w == 0) { wait_words(cx, C_XN, 8, 12u); wait_words(cx, C_RDY, 8, 12u); }
  BAR();
  const unsigned pol = (unsigned)__builtin_amdgcn_readfirstlane(__hip_atomic_load(cx.ctrl + C_POL, RLX_AGENT)) & 1u;
  topk_part(cx, IDX);
  TRG(96 + t32, 3);
  const int hf = f & 1;
  const int cb0 = x * 16 + hf * 8;
  const int ocol = 128 * x + 64 * hf + (tid & 15) * 4;
  LAS float* RD = (LAS float*)L;
#pragma unroll 1
  for (int s = f >> 1; s < NSEG; s += 4) {
    TRT(0);
    const bool last = s + 4 >= NSEG;
    if (w == 0) { wait_words(cx, C_U + s * 32, 1, 32u); wait_words(cx, C_KQ + (s >> 1) * 32, 1, 64u); if (last) wait_words(cx, C_Y, 1, 32u); }
    BAR();
    TRT(1);
    if (last && tid < 128) {
      const int sl = tid >> 4, row = IDX[sl];
      if (((row >> 5) & 3) == (f >> 1) && (row >> 5) < s) {
        const f32x4 y4 = __builtin_bit_cast(f32x4, ld_sc1(cx.ws, (unsigned)(OFF_Y + (size_t)(sl * DM + ocol) * 4)));
        const f32x4 t1 = SELT[tid * 2], xn4 = SELT[tid * 2 + 1];
        u32x2 ov; ov.x = pk2(t1[0] + bfr(xn4[0] + y4[0]), t1[1] + bfr(xn4[1] + y4[1])); ov.y = pk2(t1[2] + bfr(xn4[2] + y4[2]), t1[3] + bfr(xn4[3] + y4[3]));
        *(GAS u32x2*)(p.out + (size_t)row * DM + ocol) = ov;
      }
    }
    const unsigned ub = (unsigned)(OFF_U + ((size_t)s * NCOL + cb0 + ((l & 31) >> 3)) * U_SC) + (l & 7) * 16 + w * (4 * 5 * 128);
    u32x4 Bx[2][8];
#pragma unroll
    for (int nt = 0; nt < 2; ++nt)
#pragma unroll
      for (int u = 0; u < 8; ++u) Bx[nt][u] = ld_sc1(cx.ws, ub + nt * (4 * (unsigned)U_SC) + ((u >> 1) * 5 + 2 * (u & 1) + hi) * 128);
    {
      u32x4 t[8];
      const unsigned kb = (unsigned)(((size_t)s * NH + x * 8 + hf * 4) * 8192) + tid * 16;
#pragma unroll
      for (int i = 0; i < 4; ++i) { t[i] = ld_sc1(cx.ws, (unsigned)OFF_KX + kb + i * 8192); t[4 + i] = ld_sc1(cx.ws, (unsigned)OFF_QX + kb + i * 8192); }
#pragma unroll
      for (int i = 0; i < 8; ++i) asm volatile("" ::"v"(t[i]));
    }
    TRT(2);
    const int orow = 32 * s + (tid >> 4);
    const u32x2 xin = *(const GAS u32x2*)(p.x + (size_t)orow * DM + ocol);
    const u32x2 stat = __builtin_amdgcn_raw_buffer_load_b64(cx.ws, (unsigned)(OFF_STAT + (size_t)orow * 16), 0, AUX_SC1);
    int sel = -1;
#pragma unroll
    for (int c = 0; c < TOPC; ++c) if (IDX[c] == orow) sel = c;
    f32x4 y4 = {0.f, 0.f, 0.f, 0.f};
    if (last && sel >= 0) y4 = __builtin_bit_cast(f32x4, ld_sc1(cx.ws, (unsigned)(OFF_Y + (size_t)(sel * DM + ocol) * 4)));
    if (w == 0) {
      const unsigned fa = ex_addr(l < 32 ? FPU + l * 128 : FUMU + (cb0 + (l & 7)) * 128, pol);
      for (unsigned spins = 0;; ++spins) {
        asm volatile("" ::: "memory");
        const unsigned v = __builtin_amdgcn_raw_buffer_load_b32(cx.ex, fa, 0, AUX_SC1) + 1u;
        if (__all(v >= (unsigned)(s + 1))) break;
        if (spins > WAIT_CAP) { __hip_atomic_store(cx.ctrl + C_TMO, 1u, RLX_AGENT); break; }
        if ((spins & 63u) == 63u && __builtin_amdgcn_readfirstlane(__hip_atomic_load(cx.ctrl + C_TMO, RLX_AGENT)) == 1u) break;
        __builtin_amdgcn_s_sleep(TO_SLEEP);
      }
    }
    BAR();
    TRT(3);
    const unsigned pb = (unsigned)(OFF_PX + ((size_t)s * NH + w * 4) * 2560) + (l & 31) * 16;
    u32x4 Ax[8], Am[2], Bm[2][2];
#pragma unroll
    for (int u = 0; u < 8; ++u) Ax[u] = ld_sc1(cx.ws, pb + ((u >> 1) * 5 + 2 * (u & 1) + hi) * 512);
#pragma unroll
    for (int v = 0; v < 2; ++v) {
      Am[v] = ld_sc1(cx.ws, pb + ((2 * v + hi) * 5 + 4) * 512);
      Bm[0][v] = ld_sc1(cx.ws, ub + ((2 * v + hi) * 5 + 4) * 128);
      Bm[1][v] = ld_sc1(cx.ws, ub + 4 * (unsigned)U_SC + ((2 * v + hi) * 5 + 4) * 128);
    }
#pragma unroll
    for (int nt = 0; nt < 2; ++nt) {
      f32x16 acc = {0.f};
#pragma unroll
      for (int u = 0; u < 8; ++u) acc = mfma32(Ax[u], Bx[nt][u], acc);
#pragma unroll
      for (int v = 0; v < 2; ++v) acc = mfma32(Am[v], Bm[nt][v], acc);
#pragma unroll
      for (int r = 0; r < 16; ++r) RD[(w * 32 + (r & 3) + 8 * (r >> 2) + 4 * hi) * 68 + nt * 32 + (l & 31)] = acc[r];
    }
    BAR();
    TRT(4);
    {
      LAS float* q = RD + (tid >> 4) * 68 + (tid & 15) * 4;
      f32x4 sum = *(LAS f32x4*)q;
#pragma unroll
      for (int k = 1; k < 8; ++k) sum += *(LAS f32x4*)(q + k * 32 * 68);
      const float mb = u2f(stat.x), den = u2f(stat.y);
      f32x4 t1, xn4;
#pragma unroll
      for (int k = 0; k < 4; ++k) {
        const float xv = (k & 1) ? bfhi(xin[k >> 1]) : bflo(xin[k >> 1]);
        xn4[k] = bfr(bfr(xv - mb) / den);
        t1[k] = bfr(xv + bfr(sum[k]));
      }
      if (sel >= 0 && !last) { SELT[(sel * 16 + (tid & 15)) * 2] = t1; SELT[(sel * 16 + (tid & 15)) * 2 + 1] = xn4; }
      else {
        u32x2 ov; ov.x = pk2(t1[0] + bfr(xn4[0] + y4[0]), t1[1] + bfr(xn4[1] + y4[1])); ov.y = pk2(t1[2] + bfr(xn4[2] + y4[2]), t1[3] + bfr(xn4[3] + y4[3]));
        *(GAS u32x2*)(p.out + (size_t)orow * DM + ocol) = ov;
      }
    }
  }
}

constexpr int LDS_BYTES = 160 * 1024;

__global__ void __launch_bounds__(512, 2) k_fused(Params p) {
  extern __shared__ __attribute__((aligned(16))) unsigned char lds_raw[];
  LAS unsigned char* L = (LAS unsigned char*)lds_raw;
  const int tid = threadIdx.x;
  Ctx cx;
  cx.wave = __builtin_amdgcn_readfirstlane(tid >> 6); cx.lane = tid & 63;
  cx.ctrl = (gu32*)p.ws;
  const uintptr_t exb = ((uintptr_t)p.ws + CTRL_BYTES + 16383) & ~(uintptr_t)16383;
  cx.ex = __builtin_amdgcn_make_buffer_rsrc((void*)exb, 0, (int)EX_BYTES, 0x00020000);
  cx.exs = (u32x4){(unsigned)exb, (unsigned)(exb >> 32) & 0xffffu, (unsigned)EX_BYTES, 0x00020000u};
  cx.ws = __builtin_amdgcn_make_buffer_rsrc((void*)p.ws, 0, (int)WS_END, 0x00020000);
  cx.pol = 0;
  volatile LAS int* sh = (volatile LAS int*)(L + 155 * 1024);
  if (tid == 0) {
    const unsigned x = xcc_id() & 7u;
    const unsigned t = __hip_atomic_fetch_add(cx.ctrl + C_RC + x * 32, 1u, RLX_AGENT) + 1u;
    __hip_atomic_fetch_add(cx.ctrl + C_TOT, 1u, RLX_AGENT);
    int role;
    if (t < 32u) role = (int)(x * 32 + t);
    else {
      const unsigned o = __hip_atomic_fetch_add(cx.ctrl + C_OVF, 1u, RLX_AGENT) + 1u;
      unsigned spins = 0; role = -1;
      while (__hip_atomic_load(cx.ctrl + C_TOT, RLX_AGENT) + 1u < 256u) { if (++spins > WAIT_CAP) break; __builtin_amdgcn_s_sleep(2); }
      unsigned seen = 0;
      for (int xx = 0; xx < 8 && role < 0; ++xx) {
        const unsigned cnt = __hip_atomic_load(cx.ctrl + C_RC + xx * 32, RLX_AGENT) + 1u;
        const unsigned holes = cnt < 32u ? 32u - cnt : 0u;
        if (o < seen + holes) role = xx * 32 + (int)(cnt + (o - seen));
        seen += holes;
      }
    }
    sh[0] = role;
  }
  __syncthreads();
  const int role = __builtin_amdgcn_readfirstlane(sh[0]);
  __syncthreads();
  if (role < 0) return;
  TRG(role < 128 ? ((role & 31) < 8 ? (role >> 5) * 8 + (role & 31) : ((role & 31) < 24 ? 32 + (role >> 5) * 16 + (role & 31) - 8 : 96 + (role >> 5) * 8 + (role & 31) - 24)) : role, 0);
  const int rx = role >> 5, rt_ = role & 31;
  if (rx >= 4) {
    const int j = role - 128, h = j >> 2, k = j & 3;
    if (k < 3) proj_role(p, cx, L, h, k, j);
    else u_role(p, cx, L, h, j);
    TRG(role, 1);
    return;
  }
  if (rt_ >= 24) { to_role(p, cx, L, rx, rt_ - 24); TRG(96 + rx * 8 + rt_ - 24, 1); return; }
  if (role == 0 && cx.wave == 0) {
    unsigned best0 = ~0u, best1 = ~0u;
    for (int rep = 0; rep < 4; ++rep) {
      unsigned t0 = now32();
      if (cx.lane == 0) st_sc1_32(cx.ex, TAGU * 2, 0u);
      DRAIN();
      unsigned t1 = now32();
      best0 = min(best0, t1 - t0);
      if (cx.lane == 0) st_sc1_32(cx.ex, TAGU * 2 + 8192u, 0u);
      DRAIN();
      t0 = now32();
      best1 = min(best1, t0 - t1);
    }
    if (cx.lane == 0) __hip_atomic_store(cx.ctrl + C_POL, best1 < best0 ? 1u : 0u, RLX_AGENT);
  }
  if (cx.wave == 0) {
    wait_words(cx, C_POL, 1, 1u);
    if (cx.lane == 0) sh[1] = (int)(__hip_atomic_load(cx.ctrl + C_POL, RLX_AGENT) & 1u);
  }
  __syncthreads();
  cx.pol = (unsigned)__builtin_amdgcn_readfirstlane(sh[1]);
  __syncthreads();
  if (rt_ < 8) head_role(p, cx, L, rx * 8 + rt_);
  else col_role(p, cx, L, rx * 16 + rt_ - 8);
}

extern "C" void kernel_launch(void* const* d_in, const int* in_sizes, int n_in, void* d_out, int out_size, void* d_ws, size_t ws_size, hipStream_t stream) {
  Params p;
  p.x = (const u16*)d_in[0]; p.mem0 = (const u16*)d_in[1]; p.Wq = (const u16*)d_in[2]; p.Wk = (const u16*)d_in[3]; p.Wv = (const u16*)d_in[4]; p.Wo = (const u16*)d_in[5];
  p.w_r = (const u16*)d_in[8]; p.w_gate = (const u16*)d_in[9]; p.w_up = (const u16*)d_in[10]; p.w_down = (const u16*)d_in[11];
  p.out = (u16*)d_out; p.ws = (unsigned char*)d_ws;
  p.dh = D_H; p.dm = D_M; p.dp = D_P; p.pad = 0;
  (void)hipFuncSetAttribute((const void*)k_fused, hipFuncAttributeMaxDynamicSharedMemorySize, LDS_BYTES);
  (void)hipMemsetAsync(d_ws, 0xFF, CTRL_BYTES, stream);
  k_fused<<<256, 512, LDS_BYTES, stream>>>(p);
}
```

```cpp
#include <hip/hip_runtime.h>
#include <hip/hip_bf16.h>
#include <stdint.h>

#define GAS __attribute__((address_space(1)))
#define LAS __attribute__((address_space(3)))
typedef unsigned short u16;
typedef __bf16 bf16x8 __attribute__((ext_vector_type(8)));
typedef __bf16 bf16x16 __attribute__((ext_vector_type(16)));
typedef __bf16 bf16x2 __attribute__((ext_vector_type(2)));
typedef float f32x4 __attribute__((ext_vector_type(4)));
typedef float f32x2 __attribute__((ext_vector_type(2)));
typedef float f32x16 __attribute__((ext_vector_type(16)));
typedef unsigned u32x4 __attribute__((ext_vector_type(4)));
typedef unsigned u32x2 __attribute__((ext_vector_type(2)));
typedef unsigned u32x8 __attribute__((ext_vector_type(8)));
typedef GAS unsigned gu32;
#define RLX_AGENT __ATOMIC_RELAXED, __HIP_MEMORY_SCOPE_AGENT

constexpr int S_ = 2048, DM = 512, NH = 32, DH = 128, HD = 4096, NSEG = 64, FF = 1792, TOPC = 8;
constexpr int NCOL = 64;

constexpr size_t CTRL_BYTES = 131072;
constexpr int RM = 4, RP = 3;
constexpr unsigned MREP = 4 * 8192u, PBASE = RM * MREP, PSLOT = 3 * 8192u, PREP = 4 * PSLOT, TAGU = PBASE + RP * PREP;
constexpr unsigned FPU = TAGU + 8192u, FUMU = TAGU + 16384u;
constexpr size_t EX_BYTES = (size_t)(TAGU + 3 * 8192u) * 2;
constexpr size_t OFF_XN = 4u << 20;
constexpr size_t OFF_KX = OFF_XN + (size_t)S_ * DM * 2;
constexpr size_t OFF_QX = OFF_KX + (size_t)S_ * HD * 2;
constexpr size_t OFF_VX = OFF_QX + (size_t)S_ * HD * 2;
constexpr size_t OFF_U = OFF_VX + (size_t)S_ * HD * 2;
constexpr size_t U_SC = 160 * 128;
constexpr size_t OFF_WVO = OFF_U + (size_t)NSEG * NCOL * U_SC;
constexpr size_t OFF_PX = OFF_WVO + (size_t)NCOL * 16 * 8 * 64 * 32;
constexpr size_t OFF_STAT = OFF_PX + (size_t)NSEG * NH * 5 * 512;
constexpr size_t OFF_GU = OFF_STAT + (size_t)S_ * 16;
constexpr size_t OFF_Y = OFF_GU + TOPC * FF * 4;
constexpr size_t OFF_DBG = OFF_Y + TOPC * DM * 4;
constexpr size_t WS_END = OFF_DBG + (1u << 20);

constexpr int C_RC = 0, C_OVF = 256, C_TOT = 288, C_POL = 320, C_TMO = 352, C_GU = 384, C_Y = 416;
constexpr int C_XN = 512, C_WVO = 768, C_RDY = 1024;
constexpr int C_V = 2048;
constexpr int C_KQ = 4096, C_U = 6144;
constexpr int C_FP = 8192, C_FUM = 9216;

#define D_H 3100
#define D_M 600
#define D_P 950

#define TO_SLEEP 8
#define COL_HSLEEP 4
struct Params {
  const u16 *x, *mem0, *Wq, *Wk, *Wv, *Wo, *w_r, *w_gate, *w_up, *w_down;
  u16* out;
  unsigned char* ws;
  int dh, dm, dp, pad;
};

__device__ __forceinline__ unsigned f2u(float f) { return __builtin_bit_cast(unsigned, f); }
__device__ __forceinline__ float u2f(unsigned u) { return __builtin_bit_cast(float, u); }
__device__ __forceinline__ float bf2f(u16 b) { return u2f((unsigned)b << 16); }
__device__ __forceinline__ float bflo(unsigned u) { return u2f(u << 16); }
__device__ __forceinline__ float bfhi(unsigned u) { return u2f(u & 0xffff0000u); }
__device__ __forceinline__ u16 f2bf(float f) { __bf16 b = (__bf16)f; return __builtin_bit_cast(u16, b); }
__device__ __forceinline__ float bfr(float f) { return bf2f(f2bf(f)); }
__device__ __forceinline__ unsigned pk2(float lo, float hi) { f32x2 v = {lo, hi}; bf16x2 b = __builtin_convertvector(v, bf16x2); return __builtin_bit_cast(unsigned, b); }

constexpr int AUX_SC1 = 16;
constexpr unsigned SENT = 0xFFFFFFFFu;
constexpr unsigned SPIN_CAP = 1u << 15;
constexpr unsigned WAIT_CAP = 1u << 17;
__device__ __forceinline__ unsigned xcc_id() { return (unsigned)__builtin_amdgcn_s_getreg(20 | (3 << 11)) & 0xfu; }
__device__ __forceinline__ unsigned now32() { unsigned long long t; asm volatile("s_memtime %0\n\ts_waitcnt lgkmcnt(0)" : "=s"(t)::"memory"); return (unsigned)t; }
__device__ __forceinline__ u32x4 ld_sc1(__amdgpu_buffer_rsrc_t rs, unsigned off) { return __builtin_amdgcn_raw_buffer_load_b128(rs, off, 0, AUX_SC1); }
__device__ __forceinline__ void st_sc1(__amdgpu_buffer_rsrc_t rs, unsigned off, u32x4 v) { __builtin_amdgcn_raw_buffer_store_b128(v, rs, off, 0, AUX_SC1); }
__device__ __forceinline__ void st_sc1s(__amdgpu_buffer_rsrc_t rs, unsigned off, unsigned soff, u32x4 v) { __builtin_amdgcn_raw_buffer_store_b128(v, rs, off, soff, AUX_SC1); }
__device__ __forceinline__ void st_sc1_64(__amdgpu_buffer_rsrc_t rs, unsigned off, u32x2 v) { __builtin_amdgcn_raw_buffer_store_b64(v, rs, off, 0, AUX_SC1); }
__device__ __forceinline__ void st_sc1_32(__amdgpu_buffer_rsrc_t rs, unsigned off, unsigned v) { __builtin_amdgcn_raw_buffer_store_b32(v, rs, off, 0, AUX_SC1); }
__device__ __forceinline__ unsigned nosent(unsigned x) { return min(x, 0xFFFFFFFEu); }
__device__ __forceinline__ f32x4 mfma16(u32x4 a, u32x4 b, f32x4 c) { return __builtin_amdgcn_mfma_f32_16x16x32_bf16(__builtin_bit_cast(bf16x8, a), __builtin_bit_cast(bf16x8, b), c, 0, 0, 0); }
__device__ __forceinline__ f32x16 mfma32(u32x4 a, u32x4 b, f32x16 c) { return __builtin_amdgcn_mfma_f32_32x32x16_bf16(__builtin_bit_cast(bf16x8, a), __builtin_bit_cast(bf16x8, b), c, 0, 0, 0); }
__device__ __forceinline__ f32x4 smfma(u32x4 a, u32x8 b, f32x4 c, int idx) { return __builtin_amdgcn_smfmac_f32_16x16x64_bf16(__builtin_bit_cast(bf16x8, a), __builtin_bit_cast(bf16x16, b), c, idx, 0, 0); }
typedef short v4i16_t __attribute__((ext_vector_type(4)));
__device__ __forceinline__ u32x2 tr_read(LAS unsigned char* p) { return __builtin_bit_cast(u32x2, __builtin_amdgcn_ds_read_tr16_b64_v4i16((LAS v4i16_t*)p)); }
__device__ __forceinline__ unsigned ex_addr(unsigned i, unsigned pol) { return (i >> 13) * 16384u + (i & 8191u) + pol * 8192u; }
__device__ __forceinline__ unsigned dpp_ror8(unsigned x) { return (unsigned)__builtin_amdgcn_update_dpp(0, (int)x, 0x128, 0xf, 0xf, true); }
#define BAR() do { asm volatile("" ::: "memory"); __builtin_amdgcn_s_waitcnt(0xC07F); __builtin_amdgcn_s_barrier(); asm volatile("" ::: "memory"); } while (0)
#define DRAIN() do { asm volatile("s_waitcnt vmcnt(0)" ::: "memory"); __builtin_amdgcn_s_waitcnt(0x0F70); } while (0)
#define GLDS(src, dst) __builtin_amdgcn_global_load_lds((const GAS unsigned*)(src), (LAS unsigned*)(dst), 16, 0, 0)
#define DM_NOPS "s_nop 15\n\ts_nop 15"
#define DM_NOPS2 "s_nop 7"
#define DP_NOPS "s_nop 15\n\ts_nop 15\n\ts_nop 15\n\ts_nop 7"
#define DP_NOPS2 "s_nop 15"

#define TRG(id, k) do {} while (0)
#define TRT(k) do {} while (0)
#define TRU(k) do {} while (0)

#define TR(k) do {} while (0)
#define TR_CAL(id, o) do {} while (0)
#define TR_OUT(id) do {} while (0)
struct Ctx {
  __amdgpu_buffer_rsrc_t ex;
  u32x4 exs;
  __amdgpu_buffer_rsrc_t ws;
  gu32* ctrl;
  unsigned pol;
  int wave, lane;
};

__device__ __forceinline__ int lane_id() { int l; asm volatile("v_mbcnt_lo_u32_b32 %0, -1, 0\n\tv_mbcnt_hi_u32_b32 %0, -1, %0" : "=v"(l)); return l; }
__device__ __forceinline__ Ctx fresh(const Ctx& c) { Ctx r = c; r.lane = lane_id(); return r; }
__device__ __forceinline__ void arrive(const Ctx& cx, int word) { __hip_atomic_fetch_add(cx.ctrl + word, 1u, RLX_AGENT); }
__device__ __forceinline__ void wait_words(const Ctx& cx, int word, int n, unsigned want) {
  gu32* a = cx.ctrl + word + (cx.lane < n ? cx.lane : 0) * 32;
  for (unsigned spins = 0;; ++spins) {
    const unsigned v = __hip_atomic_load(a, RLX_AGENT) + 1u;
    if (__all(v >= want)) break;
    if (spins > WAIT_CAP) { __hip_atomic_store(cx.ctrl + C_TMO, 1u, RLX_AGENT); break; }
    if ((spins & 255u) == 255u && __builtin_amdgcn_readfirstlane(__hip_atomic_load(cx.ctrl + C_TMO, RLX_AGENT)) == 1u) break;
    __builtin_amdgcn_s_sleep(2);
  }
  __builtin_amdgcn_fence(__ATOMIC_ACQUIRE, "wavefront");
}
#define AD_UP 32
#define AD_DN 16
__device__ __forceinline__ int adapt(int d, int d0, unsigned miss, int s) {
  if (miss && s >= 4) d = min(d + AD_UP, d0 + 500);
  if ((s & 7) == 7) d -= AD_DN;
  return d;
}
__device__ __forceinline__ bool test_word(const Ctx& cx, int word, unsigned want) {
  const unsigned v = (unsigned)__builtin_amdgcn_readfirstlane(__hip_atomic_load(cx.ctrl + word, RLX_AGENT)) + 1u;
  __builtin_amdgcn_fence(__ATOMIC_ACQUIRE, "wavefront");
  return v >= want;
}
__device__ __forceinline__ unsigned get_pol(const Ctx& cx) {
  wait_words(cx, C_POL, 1, 1u);
  return (unsigned)__builtin_amdgcn_readfirstlane(__hip_atomic_load(cx.ctrl + C_POL, RLX_AGENT)) & 1u;
}
__device__ __forceinline__ void timed_wait(unsigned tref, int d) {
  while ((int)(now32() - tref) < d - 200) __builtin_amdgcn_s_sleep(1);
  while ((int)(now32() - tref) < d) {}
}

template <class HOOK>
__device__ __forceinline__ u32x2 front(const Params& p, const Ctx& cx, LAS unsigned char* MEMIMG, const u32x8 (&W)[2][8], int s, unsigned& miss, HOOK hook) {
  const int w = cx.wave, l = cx.lane;
  LAS unsigned char* L = MEMIMG;
  TR(0);
  u32x4 v, v1 = {0, 0, 0, 0}, v2 = v1, v3 = v1, v0 = v1;
  if (s == 0) {
    const u16* mp = p.mem0 + (l & 7) * DM + (8 * w + (l >> 3)) * 8;
    asm volatile("global_load_dwordx4 %0, %1, off\n\ts_waitcnt vmcnt(0)" : "=&{v[236:239]}"(v) : "v"(mp) : "memory");
  } else {
    const unsigned off = ex_addr((unsigned)(s & 3) * 8192u + w * 1024 + l * 16, cx.pol);
    unsigned t_, cnt, sr;
    asm volatile(
      "s_mov_b32 %[sr], 0\n\t"
      "buffer_load_dwordx4 v[240:243], %[off], %[rs], %[sr] offen sc1\n\t"
      "s_add_u32 %[sr], %[sr], 65536\n\t"
      "s_cmp_eq_u32 %[sr], 262144\n\t"
      "s_cselect_b32 %[sr], 0, %[sr]\n\t"
      "s_mov_b32 %[cnt], 0\n\t"
      "L%=_1:\n\t"
      "s_waitcnt vmcnt(0)\n\t"
      "v_max3_u32 %[t], v240, v241, v242\n\t"
      "v_max_u32 %[t], %[t], v243\n\t"
      "v_cmp_eq_u32 vcc, -1, %[t]\n\t"
      "s_cbranch_vccz L%=_10\n\t"
      "buffer_load_dwordx4 v[240:243], %[off], %[rs], %[sr] offen sc1\n\t"
      "s_add_u32 %[sr], %[sr], 65536\n\t"
      "s_cmp_eq_u32 %[sr], 262144\n\t"
      "s_cselect_b32 %[sr], 0, %[sr]\n\t"
      "s_add_u32 %[cnt], %[cnt], 1\n\t"
      DM_NOPS2 "\n\t"
      "s_cmp_lt_u32 %[cnt], %[cap]\n\t"
      "s_cbranch_scc1 L%=_1\n\t"
      "s_waitcnt vmcnt(0)\n\t"
      "L%=_10:\n\t"
      "v_mov_b32 v236, v240\n\t"
      "v_mov_b32 v237, v241\n\t"
      "v_mov_b32 v238, v242\n\t"
      "v_mov_b32 v239, v243\n\t"
      "L%=_20:\n\t"
      : "=&{v[240:243]}"(v0), "=&{v[244:247]}"(v1), "=&{v[248:251]}"(v2), "=&{v[252:255]}"(v3), "=&{v[236:239]}"(v), [t] "=&v"(t_), [cnt] "=&s"(cnt), [sr] "=&s"(sr)
      : [off] "v"(off), [rs] "s"(cx.exs), [cap] "s"(SPIN_CAP)
      : "vcc", "scc", "memory");
    if (cnt >= SPIN_CAP) __hip_atomic_store(cx.ctrl + C_TMO, 1u, RLX_AGENT);
    miss = cnt;
  }
  TR(1);
  LAS unsigned char* mb = MEMIMG + l * 16;
  *(LAS u32x4*)(mb + w * 1024) = v;
  const int idx = (l & 8) ? 0xEEEE : 0x4444;
  f32x4 a0 = {0.f, 0.f, 0.f, 0.f}, a1 = {0.f, 0.f, 0.f, 0.f};
  a0 = smfma(v, W[0][0], a0, idx);
  a1 = smfma(v, W[1][0], a1, idx);
  asm volatile("" : "+v"(a0), "+v"(a1));
  BAR();
  TR(2);
  u32x4 a[7];
#pragma unroll
  for (int i = 1; i < 8; ++i) a[i - 1] = *(LAS u32x4*)(mb + ((w + i) & 7) * 1024);
  __builtin_amdgcn_sched_barrier(0);
#pragma unroll
  for (int i = 1; i < 8; ++i) {
    a0 = smfma(a[i - 1], W[0][i], a0, idx);
    a1 = smfma(a[i - 1], W[1][i], a1, idx);
    if (i == 5) {
      asm volatile("" : "+v"(a0), "+v"(a1));
      hook();
      __builtin_amdgcn_sched_barrier(0);
    }
  }
  float o[4];
#pragma unroll
  for (int r = 0; r < 4; ++r) {
    auto sw = __builtin_amdgcn_permlane32_swap(f2u(a0[r]), f2u(a1[r]), false, false);
    o[r] = u2f(sw[0]) + u2f(sw[1]);
  }
  u32x2 res; res.x = pk2(o[0], o[1]); res.y = pk2(o[2], o[3]);
  return res;
}

constexpr float SCALE = 0.08838834764831845f;
constexpr float LOG2E = 1.4426950408889634f;
__device__ __forceinline__ float vmax(float a, float b) { float r; asm("v_max_f32 %0, %1, %2" : "=v"(r) : "v"(a), "v"(b)); return r; }
__device__ __forceinline__ float vmax3(float a, float b, float c) { float r; asm("v_max3_f32 %0, %1, %2, %3" : "=v"(r) : "v"(a), "v"(b), "v"(c)); return r; }
template <bool MAXOP> __device__ __forceinline__ float red_g(float v) {
  {
    auto r = __builtin_amdgcn_permlane16_swap(f2u(v), f2u(v), false, false);
    const float a = u2f(r[0]), b = u2f(r[1]);
    v = MAXOP ? vmax(a, b) : a + b;
  }
  {
    auto r = __builtin_amdgcn_permlane32_swap(f2u(v), f2u(v), false, false);
    const float a = u2f(r[0]), b = u2f(r[1]);
    v = MAXOP ? vmax(a, b) : a + b;
  }
  return v;
}

template <int ROLE>
__device__ __forceinline__ void head_loop(const Params& p, const Ctx& cx_, LAS unsigned char* L, int h, const u32x8 (&W)[2][8]) {
  const Ctx cx = fresh(cx_);
  const int w = cx.wave, l = cx.lane, g = l >> 4, n = l & 15;
  LAS unsigned char* MEMIMG = L;
  LAS unsigned char* QKIMG = L + 8192;
  LAS unsigned* XCNT = (LAS unsigned*)(L + 12288);
  LAS unsigned char* KQ = L + 16384;
  auto stage = [&](int s) {
    wait_words(cx, C_KQ + (s >> 1) * 32, 1, 64u);
    const unsigned kb = (unsigned)((w == 4 ? OFF_KX : OFF_QX) + ((size_t)s * NH + h) * 8192) + l * 16;
    LAS unsigned char* d = KQ + (s & 1) * 16384 + (w == 4 ? 0 : 8192) + l * 16;
    u32x4 pf[8];
#pragma unroll
    for (int i = 0; i < 8; ++i) pf[i] = ld_sc1(cx.ws, kb + i * 1024);
#pragma unroll
    for (int i = 0; i < 8; ++i) *(LAS u32x4*)(d + i * 1024) = pf[i];
  };
  TR_CAL(h, 0);
  if (ROLE == 2) stage(0);
  unsigned tref = 0; int Dh = p.dh;
  const int q4 = (l & 15) >> 2, pp = l & 3;
  LAS unsigned char* trb = QKIMG + (16 * (g >> 1) + 4 * (g & 1) + q4) * 32;
  const bool up = n >= 8;
  LAS unsigned char* qkw = QKIMG + (((2 * w + (l >> 5)) & 7) * 16 + n) * 32 + (w >> 2) * 16 + (g & 1) * 8;
  const unsigned pboff = PBASE + h * 640 + (n & 7) * 16 + (up ? 512 : g * 128);
  for (int s = 0; s < NSEG; ++s) {
    if (s > 0) timed_wait(tref, Dh);
    __builtin_amdgcn_s_setprio(2);
    unsigned miss = 0;
    const u32x2 qk = front(p, cx, MEMIMG, W, s, miss, [] {});
    Dh = adapt(Dh, p.dh, miss, s);
    *(LAS u32x2*)qkw = qk;
    LAS unsigned char* KI = KQ + (s & 1) * 16384 + g * 512 + n * 16;
    if (ROLE == 0) {
      u32x4 Kf[2][4];
#pragma unroll
      for (int t = 0; t < 4; ++t) { Kf[0][t] = *(LAS u32x4*)(KI + t * 2048); Kf[1][t] = *(LAS u32x4*)(KI + t * 2048 + 256); }
      unsigned paddr = ex_addr((unsigned)(s & 3) * PSLOT + pboff, cx.pol);
      asm volatile("" : "+v"(paddr));
      BAR();
      TR(3);
      __builtin_amdgcn_s_setprio(3);
      if (s == NSEG - 1) break;
      u32x4 F[4];
#pragma unroll
      for (int t = 0; t < 4; ++t) {
        const u32x2 lo = tr_read(trb + t * 1024 + pp * 8), hi = tr_read(trb + t * 1024 + 256 + pp * 8);
        F[t] = (u32x4){lo.x, lo.y, hi.x, hi.y};
      }
      __builtin_amdgcn_sched_barrier(0);
      f32x4 s0 = {0.f, 0.f, 0.f, 0.f}, s1 = s0, sm = s0;
#pragma unroll
      for (int t = 0; t < 4; ++t) { s1 = mfma16(Kf[1][t], F[t], s1); sm = mfma16(F[t], F[t], sm); s0 = mfma16(Kf[0][t], F[t], s0); }
      float v[6];
#pragma unroll
      for (int r = 0; r < 4; ++r) v[r] = u2f((unsigned)__builtin_amdgcn_update_dpp((int)f2u(s0[r]), (int)f2u(s1[r]), 0x128, 0xf, 0xc, false));
      v[4] = u2f((unsigned)__builtin_amdgcn_update_dpp((int)f2u(sm[0]), (int)f2u(sm[2]), 0x128, 0xf, 0xc, false));
      v[5] = u2f((unsigned)__builtin_amdgcn_update_dpp((int)f2u(sm[1]), (int)f2u(sm[3]), 0x128, 0xf, 0xc, false));
      v[4] = (g >= 2) ? v[4] : -INFINITY;
      v[5] = (g >= 2) ? v[5] : -INFINITY;
      float mx = vmax3(vmax3(v[0], v[1], v[2]), v[3], vmax(v[4], v[5]));
      mx = vmax(mx, u2f(dpp_ror8(f2u(mx))));
      mx = red_g<true>(mx);
      const float cs = SCALE * LOG2E, mc = -mx * cs;
#pragma unroll
      for (int i = 0; i < 6; ++i) v[i] = __builtin_amdgcn_exp2f(__builtin_fmaf(v[i], cs, mc));
      float sum = ((v[0] + v[1]) + (v[2] + v[3])) + (v[4] + v[5]);
      sum += u2f(dpp_ror8(f2u(sum)));
      sum = red_g<false>(sum);
      const float inv = __builtin_amdgcn_rcpf(sum);
      const unsigned px0 = nosent(pk2(v[0] * inv, v[1] * inv)), px1 = nosent(pk2(v[2] * inv, v[3] * inv));
      const unsigned pm = nosent(pk2(v[4] * inv, v[5] * inv));
      const unsigned o0 = dpp_ror8(px0), o1 = dpp_ror8(px1), t1 = dpp_ror8(pm);
      const unsigned d2 = __builtin_amdgcn_permlane16_swap(t1, t1, false, false)[1], d3 = __builtin_amdgcn_permlane16_swap(pm, pm, false, false)[1];
      u32x4 dat; dat.x = up ? t1 : px0; dat.y = up ? pm : px1; dat.z = up ? d2 : o0; dat.w = up ? d3 : o1;
      if (!up || g == 2) {
#pragma unroll
        for (int r = 0; r < RP; ++r) st_sc1s(cx.ex, paddr, r * 2 * PREP, dat);
      }
      TR(4);
      __builtin_amdgcn_s_setprio(0);
      if (l < 40) {
        const unsigned ra = ex_addr(PBASE + (unsigned)((s + 2) & 3) * PSLOT + h * 640 + l * 16, cx.pol);
#pragma unroll
        for (int r = 0; r < RP; ++r) st_sc1s(cx.ex, ra, r * 2 * PREP, (u32x4){SENT, SENT, SENT, SENT});
      }
      tref = (unsigned)__builtin_amdgcn_readfirstlane(*(volatile LAS unsigned*)(XCNT + 1));
    } else {
      BAR();
      TR(3);
      tref = now32();
      if (ROLE == 1 && w == 1 && l == 0) XCNT[1] = tref;
      __builtin_amdgcn_s_setprio(0);
      if (ROLE == 2) {
        __builtin_amdgcn_s_sleep(3);
        if (s + 1 < NSEG) stage(s + 1);
      } else if (ROLE == 3) {
        __builtin_amdgcn_s_sleep(3);
        const int qt = w - 5;
        u32x4 F[4], Kf[2][4], Qf[4];
#pragma unroll
        for (int t = 0; t < 4; ++t) {
          const u32x2 lo = tr_read(trb + t * 1024 + pp * 8), hi = tr_read(trb + t * 1024 + 256 + pp * 8);
          F[t] = (u32x4){lo.x, lo.y, hi.x, hi.y};
          Kf[0][t] = *(LAS u32x4*)(KI + t * 2048); Kf[1][t] = *(LAS u32x4*)(KI + t * 2048 + 256);
          Qf[t] = *(LAS u32x4*)(KI + 8192 + t * 2048 + qt * 256);
        }
        f32x4 s0 = {0.f, 0.f, 0.f, 0.f}, s1 = s0, sm = s0;
#pragma unroll
        for (int t = 0; t < 4; ++t) { s0 = mfma16(Kf[0][t], Qf[t], s0); s1 = mfma16(Kf[1][t], Qf[t], s1); sm = mfma16(F[t], Qf[t], sm); }
        const int qi = qt * 16 + n;
        float sc[12];
#pragma unroll
        for (int r = 0; r < 4; ++r) {
          sc[r] = (4 * g + r <= qi) ? s0[r] : -INFINITY;
          sc[4 + r] = (16 + 4 * g + r <= qi) ? s1[r] : -INFINITY;
          sc[8 + r] = (g >= 2) ? sm[r] : -INFINITY;
        }
        float mx = vmax3(vmax3(sc[0], sc[1], sc[2]), vmax3(sc[3], sc[4], sc[5]), vmax3(vmax3(sc[6], sc[7], sc[8]), vmax3(sc[9], sc[10], sc[11]), sc[0]));
        mx = red_g<true>(mx);
        const float cs = SCALE * LOG2E, mc = -mx * cs;
        float sum = 0.f;
#pragma unroll
        for (int i = 0; i < 12; ++i) { sc[i] = __builtin_amdgcn_exp2f(__builtin_fmaf(sc[i], cs, mc)); sum += sc[i]; }
        sum = red_g<false>(sum);
        const float inv = __builtin_amdgcn_rcpf(sum);
        u32x4 px; px.x = pk2(sc[0] * inv, sc[1] * inv); px.y = pk2(sc[2] * inv, sc[3] * inv); px.z = pk2(sc[4] * inv, sc[5] * inv); px.w = pk2(sc[6] * inv, sc[7] * inv);
        u32x2 pm; pm.x = pk2(sc[8] * inv, sc[9] * inv); pm.y = pk2(sc[10] * inv, sc[11] * inv);
        const unsigned pb = (unsigned)(OFF_PX + ((size_t)s * NH + h) * 2560) + qi * 16;
        st_sc1(cx.ws, pb + g * 512, px);
        if (g >= 2) st_sc1_64(cx.ws, pb + 4 * 512 + (g - 2) * 8, pm);
        DRAIN();
        if (l == 0) {
          const unsigned old = __hip_atomic_fetch_add(XCNT, 1u, __ATOMIC_RELAXED, __HIP_MEMORY_SCOPE_WORKGROUP);
          if (old & 1u) st_sc1_32(cx.ex, ex_addr(FPU + h * 128, cx.pol), (unsigned)s);
        }
      }
      TR(5);
    }
  }
  TR_OUT(h);
}

__device__ __forceinline__ void chain_ready(const Ctx& cx_, int id) {
  const Ctx cx = fresh(cx_);
  if (cx.wave == 0) wait_words(cx, C_RDY, 8, 12u);
  BAR();
}

__device__ __forceinline__ void head_role(const Params& p, const Ctx& cx_, LAS unsigned char* L, int h) {
  Ctx cx = fresh(cx_);
  const int w = cx.wave, l = cx.lane, g = l >> 4, tid = w * 64 + l;
  u32x8 W[2][8];
#pragma unroll
  for (int rd = 0; rd < 2; ++rd) {
    const u16* Wsrc = rd ? p.Wk : p.Wq;
#pragma unroll
    for (int i = 0; i < 16; ++i) {
      const int kap = i * 4 + (l >> 4), sig = l & 15;
      const int gsw = (kap & 3) | (((kap >> 3) & 1) << 2);
      const int chunk = (((sig >> 1) ^ gsw) << 1) | (sig & 1);
      const int d = 64 * w + 16 * (kap >> 4) + 8 * ((kap >> 1) & 1) + 2 * ((kap >> 2) & 3) + (kap & 1);
      GLDS(Wsrc + (size_t)d * HD + h * DH + chunk * 8, L + w * 16384 + i * 1024);
    }
    if (rd == 0) {
      cx.pol = get_pol(cx);
      if (tid == 511) st_sc1_32(cx.ex, ex_addr(FPU + h * 128, cx.pol), SENT);
#pragma unroll
      for (int k = 0; k < 2; ++k) {
        const int q = tid + k * 512;
        if (q < 4 * RP * 40) {
          const int pc = q % 40, sr = q / 40;
          st_sc1(cx.ex, ex_addr(PBASE + (unsigned)(sr >> 2) * PREP + (unsigned)(sr & 3) * PSLOT + h * 640 + pc * 16, cx.pol), (u32x4){SENT, SENT, SENT, SENT});
        }
      }
    }
    DRAIN();
    BAR();
    if (rd == 0 && tid == 0) arrive(cx, C_RDY + (h & 7) * 32);
    if ((w >> 2) == rd) {
      const int q = (l & 15) >> 2, pp = l & 3;
#pragma unroll
      for (int i = 0; i < 8; ++i) {
        LAS unsigned char* img = L + ((w + i) & 7) * 16384;
#pragma unroll
        for (int t = 0; t < 2; ++t) {
          const int et = (2 * w + t) & 7;
          u32x8 f;
#pragma unroll
          for (int jq = 0; jq < 4; ++jq) {
            const int kap = (jq >> 1) * 32 + 8 * g + (jq & 1) * 4 + q;
            const int gsw = (kap & 3) | (((kap >> 3) & 1) << 2);
            const u32x2 v = tr_read(img + kap * 256 + ((et ^ gsw) * 32) + pp * 8);
            f[jq * 2] = v.x; f[jq * 2 + 1] = v.y;
          }
          W[t][i] = f;
        }
      }
    }
    BAR();
  }
  if (tid == 0) *(LAS unsigned*)(L + 12288) = 0u;
  TRG(h, 1);
  chain_ready(cx, h);
  TRG(h, 2);
  if (w == 0) head_loop<0>(p, cx, L, h, W);
  else if (w < 4) head_loop<1>(p, cx, L, h, W);
  else if (w == 4 || w == 7) head_loop<2>(p, cx, L, h, W);
  else head_loop<3>(p, cx, L, h, W);
}

__device__ __forceinline__ void wo_issue(const Params& p, const Ctx& cx_, LAS unsigned char* L, int pid) {
  const Ctx cx = fresh(cx_);
  const int w = cx.wave, l = cx.lane, h = pid & 31, qr = pid >> 5;
#pragma unroll
  for (int i = 0; i < 4; ++i) {
    const int r = (w * 4 + i) * 4 + (l >> 4);
    GLDS(p.Wv + (size_t)(128 * qr + r) * HD + h * DH + ((l & 15) ^ (r & 15)) * 8, L + 131072 + (w * 4 + i) * 1024);
  }
#pragma unroll
  for (int i = 0; i < 16; ++i) {
    const int e = w * 16 + i;
    GLDS(p.Wo + ((size_t)h * DH + e) * DM + ((((l >> 2) ^ (i & 3)) << 2) + (l & 3)) * 8, L + e * 1024);
  }
}
__device__ __forceinline__ void wvo_part(const Params& p, const Ctx& cx_, LAS unsigned char* L, int pid, u32x4 (&B)[2][8]) {
  const Ctx cx = fresh(cx_);
  const int w = cx.wave, l = cx.lane, hi = l >> 5, h = pid & 31, qr = pid >> 5;
  {
    const int blk = (l >> 4) & 1, q = (l & 15) >> 2, pp = l & 3;
#pragma unroll
    for (int nt = 0; nt < 2; ++nt) {
      LAS unsigned char* b0 = L + (4 * hi + q) * 1024 + ((2 * w + nt) ^ q) * 64 + blk * 32 + pp * 8;
#pragma unroll
      for (int u = 0; u < 8; ++u) {
        const u32x2 lo = tr_read(b0 + u * 16384), hi2 = tr_read(b0 + u * 16384 + 8192);
        B[nt][u] = (u32x4){lo.x, lo.y, hi2.x, hi2.y};
      }
    }
  }
#pragma unroll
  for (int i = 0; i < 4; ++i) {
    const int dt = qr * 4 + i;
    u32x4 A[8];
    LAS unsigned char* a0 = L + 131072 + (32 * i + (l & 31)) * 256 + hi * 8;
#pragma unroll
    for (int u = 0; u < 8; ++u) {
      const u32x2 lo = *(LAS u32x2*)(a0 + (((2 * u) ^ (l & 15)) << 4)), h2 = *(LAS u32x2*)(a0 + (((2 * u + 1) ^ (l & 15)) << 4));
      A[u] = (u32x4){lo.x, lo.y, h2.x, h2.y};
    }
#pragma unroll
    for (int nt = 0; nt < 2; ++nt) {
      f32x16 acc = {0.f};
#pragma unroll
      for (int u = 0; u < 8; ++u) acc = mfma32(A[u], B[nt][u], acc);
      const int col = 64 * w + 32 * nt + (l & 31);
#pragma unroll
      for (int b16 = 0; b16 < 2; ++b16) {
        const int d16 = 2 * dt + b16, T = d16 >> 2, gA = d16 & 3;
        const int lane2 = (2 * (gA & 1) + hi) * 16 + (h & 1) * 8 + (col & 7);
        u32x4 o;
        o.x = pk2(acc[8 * b16 + 0], acc[8 * b16 + 1]); o.y = pk2(acc[8 * b16 + 4], acc[8 * b16 + 5]);
        o.z = pk2(acc[8 * b16 + 2], acc[8 * b16 + 3]); o.w = pk2(acc[8 * b16 + 6], acc[8 * b16 + 7]);
        st_sc1(cx.ws, (unsigned)(OFF_WVO + ((((size_t)(col >> 3) * 16 + (h >> 1)) * 8 + T) * 2048) + (gA >> 1) * 1024 + lane2 * 16), o);
      }
    }
  }
  TRG(pid < 32 ? 128 + pid * 4 + 3 : (pid < 64 ? 64 + pid : pid - 32), 5);
  DRAIN();
  BAR();
  if (w == 0 && l == 0) arrive(cx, C_WVO + (pid & 7) * 32);
}

template <int ROLE>
__device__ __forceinline__ void col_loop(const Params& p, const Ctx& cx_, LAS unsigned char* L, int c, const u32x8 (&W)[2][8]) {
  const Ctx cx = fresh(cx_);
  const int w = cx.wave, l = cx.lane, g = l >> 4, n = l & 15, tid = w * 64 + l;
  LAS unsigned char* MEMIMG = L;
  LAS unsigned* UCNT = (LAS unsigned*)(L + 8192);
  LAS unsigned char* RED = L + 9216;
  LAS unsigned char* UIMG = L + 12288;
  const int ht = tid - 256;
  unsigned uoff[4];
#pragma unroll
  for (int i = 0; i < 4; ++i) { const int ch4 = (ht >> 3) + 32 * i; uoff[i] = (unsigned)((((ch4 >> 2) * 5 + (ch4 & 3)) * 8 + (ht & 7)) * 16); }
  u32x4 ur[4];
  auto uload = [&](int s) {
    const unsigned base = (unsigned)(OFF_U + ((size_t)s * NCOL + c) * U_SC);
#pragma unroll
    for (int i = 0; i < 4; ++i) ur[i] = ld_sc1(cx.ws, base + uoff[i]);
  };
  auto ustore = [&](int s) {
    LAS unsigned char* UI = UIMG + (s & 1) * 20480;
#pragma unroll
    for (int i = 0; i < 4; ++i) *(LAS u32x4*)(UI + uoff[i]) = ur[i];
  };
  TR_CAL(32 + c, 0);
  if (ROLE == 2) { wait_words(cx, C_U, 1, 32u); uload(0); ustore(0); }
  unsigned tb1 = 0, tpub = 0; int Dp = p.dp; bool unext = false;
  const unsigned umo2 = (unsigned)((((ht >> 3) * 5 + 4) * 8 + (ht & 7)) * 16);
  const unsigned umo = (unsigned)((((2 * (2 * w + (l >> 5)) + (n >> 3)) * 5 + 4) * 8 + (n & 7)) * 16 + (g & 1) * 8);
  for (int s = 0; s < NSEG; ++s) {
    LAS unsigned char* UI = UIMG + (s & 1) * 20480;
    u32x4 A0[5], A1[5], A2[5];
    unsigned miss = 0;
    u32x4 umprev = {0, 0, 0, 0};
    if (ROLE == 2 && s > 0) umprev = *(LAS u32x4*)(UIMG + ((s - 1) & 1) * 20480 + umo2);
    if (s > 0) timed_wait(tpub, p.dm);
    const u32x2 um = front(p, cx, MEMIMG, W, s, miss, [&] {
      if (ROLE == 2) {
        if (unext && s + 1 < NSEG - 1) uload(s + 1);
        if (s > 0) st_sc1(cx.ws, (unsigned)(OFF_U + ((size_t)(s - 1) * NCOL + c) * U_SC) + umo2, umprev);
      }
    });
    *(LAS u32x2*)(UI + umo) = um;
    BAR();
    TR(3);
    if (ROLE != 2) tb1 = now32();
    if (ROLE == 2) {
      if (s == NSEG - 1) st_sc1(cx.ws, (unsigned)(OFF_U + ((size_t)s * NCOL + c) * U_SC) + umo2, *(LAS u32x4*)(UI + umo2));
      if (!unext && s + 1 < NSEG - 1) { wait_words(cx, C_U + (s + 1) * 32, 1, 32u); uload(s + 1); }
      DRAIN();
      if (s == NSEG - 1) {
        if (l == 0) {
          const unsigned old = __hip_atomic_fetch_add(UCNT, 1u, __ATOMIC_RELAXED, __HIP_MEMORY_SCOPE_WORKGROUP);
          if (old == 3u) st_sc1_32(cx.ex, ex_addr(FUMU + c * 128, cx.pol), (unsigned)s);
        }
        break;
      }
      if (s + 1 < NSEG - 1) ustore(s + 1);
      unext = (s + 2 < NSEG - 1) && test_word(cx, C_U + (s + 2) * 32, 32u);
      TR(5);
    } else {
      if (s == NSEG - 1) break;
      u32x4 B[5];
      unsigned aoff[5];
#pragma unroll
      for (int i = 0; i < 5; ++i) {
        const int ch = (n >> 3) * 80 + 4 * (w * 5 + i) + g;
        B[i] = *(LAS u32x4*)(UI + (ch * 8 + (n & 7)) * 16);
        aoff[i] = ex_addr(PBASE + (unsigned)(s & 3) * PSLOT + ch * 128 + (n & 7) * 16, cx.pol);
      }
      timed_wait(tb1, Dp);
      __builtin_amdgcn_s_setprio(3);
      TR(4);
      f32x4 acc; unsigned t_, cnt, sr;
      asm volatile(
        "s_mov_b32 %[sr], 0\n\t"
        "buffer_load_dwordx4 v[196:199], %[o0], %[rs], %[sr] offen sc1\n\t"
        "buffer_load_dwordx4 v[200:203], %[o1], %[rs], %[sr] offen sc1\n\t"
        "buffer_load_dwordx4 v[204:207], %[o2], %[rs], %[sr] offen sc1\n\t"
        "buffer_load_dwordx4 v[208:211], %[o3], %[rs], %[sr] offen sc1\n\t"
        "buffer_load_dwordx4 v[212:215], %[o4], %[rs], %[sr] offen sc1\n\t"
        "s_add_u32 %[sr], %[sr], 196608\n\t"
        "s_cmp_eq_u32 %[sr], 589824\n\t"
        "s_cselect_b32 %[sr], 0, %[sr]\n\t"
        "s_mov_b32 %[cnt], 0\n\t"
        "L%=_1:\n\t"
        "s_waitcnt vmcnt(4)\n\t"
        "v_mfma_f32_16x16x32_bf16 %[acc], v[196:199], %[b0], 0\n\t"
        "v_max3_u32 %[t], v196, v197, v198\n\t"
        "v_max_u32 %[t], %[t], v199\n\t"
        "s_waitcnt vmcnt(3)\n\t"
        "v_mfma_f32_16x16x32_bf16 %[acc], v[200:203], %[b1], %[acc]\n\t"
        "v_max3_u32 %[t], %[t], v200, v201\n\t"
        "v_max3_u32 %[t], %[t], v202, v203\n\t"
        "s_waitcnt vmcnt(2)\n\t"
        "v_mfma_f32_16x16x32_bf16 %[acc], v[204:207], %[b2], %[acc]\n\t"
        "v_max3_u32 %[t], %[t], v204, v205\n\t"
        "v_max3_u32 %[t], %[t], v206, v207\n\t"
        "s_waitcnt vmcnt(1)\n\t"
        "v_mfma_f32_16x16x32_bf16 %[acc], v[208:211], %[b3], %[acc]\n\t"
        "v_max3_u32 %[t], %[t], v208, v209\n\t"
        "v_max3_u32 %[t], %[t], v210, v211\n\t"
        "s_waitcnt vmcnt(0)\n\t"
        "v_mfma_f32_16x16x32_bf16 %[acc], v[212:215], %[b4], %[acc]\n\t"
        "v_max3_u32 %[t], %[t], v212, v213\n\t"
        "v_max3_u32 %[t], %[t], v214, v215\n\t"
        "v_cmp_eq_u32 vcc, -1, %[t]\n\t"
        "s_cbranch_vccz L%=_20\n\t"
        "buffer_load_dwordx4 v[196:199], %[o0], %[rs], %[sr] offen sc1\n\t"
        "buffer_load_dwordx4 v[200:203], %[o1], %[rs], %[sr] offen sc1\n\t"
        "buffer_load_dwordx4 v[204:207], %[o2], %[rs], %[sr] offen sc1\n\t"
        "buffer_load_dwordx4 v[208:211], %[o3], %[rs], %[sr] offen sc1\n\t"
        "buffer_load_dwordx4 v[212:215], %[o4], %[rs], %[sr] offen sc1\n\t"
        "s_add_u32 %[sr], %[sr], 196608\n\t"
        "s_cmp_eq_u32 %[sr], 589824\n\t"
        "s_cselect_b32 %[sr], 0, %[sr]\n\t"
        "s_add_u32 %[cnt], %[cnt], 1\n\t"
        DP_NOPS2 "\n\t"
        "s_cmp_lt_u32 %[cnt], %[cap]\n\t"
        "s_cbranch_scc1 L%=_1\n\t"
        "s_waitcnt vmcnt(0)\n\t"
        "L%=_20:\n\t"
        "s_nop 7\n\t"
        : "=&{v[196:199]}"(A0[0]), "=&{v[200:203]}"(A0[1]), "=&{v[204:207]}"(A0[2]), "=&{v[208:211]}"(A0[3]), "=&{v[212:215]}"(A0[4]), "=&{v[216:219]}"(A1[0]), "=&{v[220:223]}"(A1[1]), "=&{v[224:227]}"(A1[2]), "=&{v[228:231]}"(A1[3]), "=&{v[232:235]}"(A1[4]), "=&{v[236:239]}"(A2[0]), "=&{v[240:243]}"(A2[1]), "=&{v[244:247]}"(A2[2]), "=&{v[248:251]}"(A2[3]), "=&{v[252:255]}"(A2[4]), [acc] "=&v"(acc), [t] "=&v"(t_), [cnt] "=&s"(cnt), [sr] "=&s"(sr)
        : [o0] "v"(aoff[0]), [o1] "v"(aoff[1]), [o2] "v"(aoff[2]), [o3] "v"(aoff[3]), [o4] "v"(aoff[4]), [b0] "v"(B[0]), [b1] "v"(B[1]), [b2] "v"(B[2]), [b3] "v"(B[3]), [b4] "v"(B[4]),
          [rs] "s"(cx.exs), [cap] "s"(SPIN_CAP)
        : "vcc", "scc", "memory");
      if (cnt >= SPIN_CAP) __hip_atomic_store(cx.ctrl + C_TMO, 1u, RLX_AGENT);
      TR(5);
      if ((n >> 3) == (g >> 1)) *(LAS f32x4*)(RED + (((w * 2 + (g >> 1)) * 8 + (n & 7)) * 8 + 4 * (g & 1)) * 4) = acc;
      Dp = adapt(Dp, p.dp, cnt, s);
    }
    BAR();
    TR(6);
    tpub = (unsigned)__builtin_amdgcn_s_memtime();
    if (ROLE == 2 && s > 0 && tid == 256) st_sc1_32(cx.ex, ex_addr(FUMU + c * 128, cx.pol), (unsigned)(s - 1));
    if (ROLE == 0) {
      const int m = l >> 3, c8 = l & 7;
      float x[8];
#pragma unroll
      for (int sl = 0; sl < 8; ++sl) x[sl] = *(LAS float*)(RED + ((sl * 8 + c8) * 8 + m) * 4);
      const float v = ((x[0] + x[1]) + (x[2] + x[3])) + ((x[4] + x[5]) + (x[6] + x[7]));
      const float o = u2f(__builtin_amdgcn_update_dpp(0, f2u(v), 0xB1, 0xf, 0xf, true));
      const unsigned d = nosent(pk2(v, o));
      u32x4 dat; dat.x = d;
      dat.y = (unsigned)__builtin_amdgcn_update_dpp(0, (int)d, 0xAA, 0xf, 0xf, true);
      dat.z = (unsigned)__builtin_amdgcn_update_dpp(0, (int)d, 0x104, 0xf, 0xf, true);
      dat.w = (unsigned)__builtin_amdgcn_update_dpp(0, (int)dat.y, 0x104, 0xf, 0xf, true);
      if (c8 == 0) {
        const unsigned ma = ex_addr((unsigned)((s + 1) & 3) * 8192u + c * 128 + m * 16, cx.pol);
#pragma unroll
        for (int r = 0; r < RM; ++r) st_sc1s(cx.ex, ma, r * 2 * MREP, dat);
      }
      TR(7);
      if (l < 8 * RM) st_sc1(cx.ex, ex_addr((unsigned)((s + 3) & 3) * 8192u + c * 128 + (l & 7) * 16, cx.pol) + (l >> 3) * 2 * MREP, (u32x4){SENT, SENT, SENT, SENT});
    }
    __builtin_amdgcn_s_setprio(0);
    if (ROLE == 0) asm volatile("s_waitcnt vmcnt(5)" : "+{v[196:199]}"(A0[0]), "+{v[200:203]}"(A0[1]), "+{v[204:207]}"(A0[2]), "+{v[208:211]}"(A0[3]), "+{v[212:215]}"(A0[4]), "+{v[216:219]}"(A1[0]), "+{v[220:223]}"(A1[1]), "+{v[224:227]}"(A1[2]), "+{v[228:231]}"(A1[3]), "+{v[232:235]}"(A1[4]), "+{v[236:239]}"(A2[0]), "+{v[240:243]}"(A2[1]), "+{v[244:247]}"(A2[2]), "+{v[248:251]}"(A2[3]), "+{v[252:255]}"(A2[4]) :: "memory");
    if (ROLE == 1) asm volatile("s_waitcnt vmcnt(0)" : "+{v[196:199]}"(A0[0]), "+{v[200:203]}"(A0[1]), "+{v[204:207]}"(A0[2]), "+{v[208:211]}"(A0[3]), "+{v[212:215]}"(A0[4]), "+{v[216:219]}"(A1[0]), "+{v[220:223]}"(A1[1]), "+{v[224:227]}"(A1[2]), "+{v[228:231]}"(A1[3]), "+{v[232:235]}"(A1[4]), "+{v[236:239]}"(A2[0]), "+{v[240:243]}"(A2[1]), "+{v[244:247]}"(A2[2]), "+{v[248:251]}"(A2[3]), "+{v[252:255]}"(A2[4]) :: "memory");
  }
  TR_OUT(32 + c);
}

__device__ __forceinline__ void col_role(const Params& p, const Ctx& cx0, LAS unsigned char* L, int c) {
  const int w = cx0.wave;
  Ctx cx_ = cx0;
  {
    u32x4 B[2][8];
    wo_issue(p, cx_, L, 64 + c);
    cx_.pol = get_pol(fresh(cx_));
    if (w < RM / 2) {
      const Ctx cx = fresh(cx_);
      const int tid = w * 64 + cx.lane;
      const int pc = tid & 7, sr = tid >> 3;
      if (tid == 0) st_sc1_32(cx.ex, ex_addr(FUMU + c * 128, cx.pol), SENT);
      st_sc1(cx.ex, ex_addr((unsigned)(sr >> 2) * MREP + (unsigned)(sr & 3) * 8192u + c * 128 + pc * 16, cx.pol), (u32x4){SENT, SENT, SENT, SENT});
    }
    DRAIN();
    BAR();
    { const Ctx cx = fresh(cx_); TRG(32 + c, 4); if (w == 0 && cx.lane == 0) arrive(cx, C_RDY + (c & 7) * 32); }
    wvo_part(p, cx_, L, 64 + c, B);
  }
  const Ctx cx = fresh(cx_);
  const int l = cx.lane, tid = w * 64 + l;
  TRG(32 + c, 1);
  if (w == 0) wait_words(cx, C_WVO, 8, 16u);
  BAR();
  TRG(32 + c, 2);
  u32x8 W[2][8];
#pragma unroll
  for (int t = 0; t < 2; ++t)
#pragma unroll
    for (int i = 0; i < 8; ++i) {
      const unsigned off = (unsigned)(OFF_WVO + (((size_t)c * 16 + 2 * w + t) * 8 + ((w + i) & 7)) * 2048) + l * 16;
      const u32x4 a = ld_sc1(cx.ws, off), b = ld_sc1(cx.ws, off + 1024);
      W[t][i] = (u32x8){a.x, a.y, a.z, a.w, b.x, b.y, b.z, b.w};
    }
  if (tid == 0) *(LAS unsigned*)(L + 8192) = 0u;
  chain_ready(cx, c);
  TRG(32 + c, 3);
  if (w == 0) col_loop<0>(p, cx, L, c, W);
  else if (w < 4) col_loop<1>(p, cx, L, c, W);
  else col_loop<2>(p, cx, L, c, W);
}

template <int CTRL, int RMASK> __device__ __forceinline__ unsigned dppu(unsigned old, unsigned x) { return (unsigned)__builtin_amdgcn_update_dpp((int)old, (int)x, CTRL, RMASK, 0xf, false); }
__device__ __forceinline__ float wave_sum(float v) {
  v += u2f(dppu<0x111, 0xf>(0u, f2u(v)));
  v += u2f(dppu<0x112, 0xf>(0u, f2u(v)));
  v += u2f(dppu<0x114, 0xf>(0u, f2u(v)));
  v += u2f(dppu<0x118, 0xf>(0u, f2u(v)));
  v += u2f(dppu<0x142, 0xa>(0u, f2u(v)));
  v += u2f(dppu<0x143, 0xc>(0u, f2u(v)));
  return u2f((unsigned)__builtin_amdgcn_readlane((int)f2u(v), 63));
}
__device__ __forceinline__ unsigned wave_umax(unsigned v) {
  v = max(v, dppu<0x111, 0xf>(0u, v));
  v = max(v, dppu<0x112, 0xf>(0u, v));
  v = max(v, dppu<0x114, 0xf>(0u, v));
  v = max(v, dppu<0x118, 0xf>(0u, v));
  v = max(v, dppu<0x142, 0xa>(0u, v));
  v = max(v, dppu<0x143, 0xc>(0u, v));
  return (unsigned)__builtin_amdgcn_readlane((int)v, 63);
}
template <int NR>
__device__ __forceinline__ void ln_part(const Params& p, const Ctx& cx_, int row0, int id) {
  const Ctx cx = fresh(cx_);
  const int w = cx.wave, l = cx.lane;
  const u32x4 wr = *(const GAS u32x4*)(p.w_r + l * 8);
  u32x4 xv[NR];
#pragma unroll
  for (int k = 0; k < NR; ++k) xv[k] = *(const GAS u32x4*)(p.x + (size_t)(row0 + NR * w + k) * DM + l * 8);
#pragma unroll
  for (int k = 0; k < NR; ++k) {
    const int row = row0 + NR * w + k;
    float v[8];
#pragma unroll
    for (int i = 0; i < 4; ++i) { v[2 * i] = bflo(xv[k][i]); v[2 * i + 1] = bfhi(xv[k][i]); }
    const float mean = wave_sum(((v[0] + v[1]) + (v[2] + v[3])) + ((v[4] + v[5]) + (v[6] + v[7]))) * (1.f / DM);
    float q = 0.f;
#pragma unroll
    for (int i = 0; i < 8; ++i) { const float d = v[i] - mean; q += d * d; }
    const float mb = bfr(mean), vb = bfr(wave_sum(q) * (1.f / DM));
    const float den = bfr(sqrtf(bfr(vb + 1e-5f)));
    float r = 0.f;
#pragma unroll
    for (int i = 0; i < 8; ++i) v[i] = bfr(bfr(v[i] - mb) / den);
    u32x4 o; o.x = pk2(v[0], v[1]); o.y = pk2(v[2], v[3]); o.z = pk2(v[4], v[5]); o.w = pk2(v[6], v[7]);
    st_sc1(cx.ws, (unsigned)(OFF_XN + (size_t)row * 1024) + l * 16, o);
#pragma unroll
    for (int i = 0; i < 4; ++i) r += v[2 * i] * bflo(wr[i]) + v[2 * i + 1] * bfhi(wr[i]);
    r = bfr(wave_sum(r));
    if (l == 0) st_sc1(cx.ws, (unsigned)(OFF_STAT + (size_t)row * 16), (u32x4){f2u(mb), f2u(den), f2u(r), 0u});
  }
  DRAIN();
  BAR();
  if (w == 0 && l == 0) arrive(cx, C_XN + (id & 7) * 32);
}
__device__ __forceinline__ void stage_w(const u16* Wsrc, int h, const Ctx& cx, LAS unsigned char* L) {
  const int w = cx.wave, l = cx.lane;
#pragma unroll
  for (int i = 0; i < 16; ++i) {
    const int row = w * 64 + i * 4 + (l >> 4), sig = l & 15;
    const int chunk = (((sig >> 1) ^ ((row & 3) << 1)) << 1) | (sig & 1);
    GLDS(Wsrc + (size_t)row * HD + h * DH + chunk * 8, L + w * 16384 + i * 1024);
  }
}
__device__ __forceinline__ void read_wt(const Ctx& cx, LAS unsigned char* L, int nt, u32x4 (&A)[32]) {
  const int l = cx.lane, hi = l >> 5, blk = (l >> 4) & 1, q = (l & 15) >> 2, pp = l & 3;
  LAS unsigned char* b0 = L + (8 * hi + q) * 256 + (((2 * nt + blk) ^ (q << 1)) * 32) + pp * 8;
#pragma unroll
  for (int u = 0; u < 32; ++u) {
    const u32x2 lo = tr_read(b0 + u * 4096), h2 = tr_read(b0 + u * 4096 + 1024);
    A[u] = (u32x4){lo.x, lo.y, h2.x, h2.y};
  }
}
constexpr int XSTR = 1040;
__device__ __forceinline__ void xn_ready(const Ctx& cx_) {
  const Ctx cx = fresh(cx_);
  if (cx.wave == 0) {
    wait_words(cx, C_XN, 8, 12u);
    __builtin_amdgcn_fence(__ATOMIC_ACQUIRE, "agent");
    DRAIN();
  }
  BAR();
}
__device__ __forceinline__ void proj_tile(const Ctx& cx, LAS unsigned char* xt, const u32x4 (&A)[32], unsigned dst) {
  const int l = cx.lane, hi = l >> 5;
  LAS unsigned char* b = xt + (l & 31) * XSTR + hi * 16;
  f32x16 acc = {0.f};
#pragma unroll
  for (int u = 0; u < 32; ++u) acc = mfma32(A[u], *(LAS u32x4*)(b + u * 32), acc);
#pragma unroll
  for (int q = 0; q < 2; ++q) {
    u32x4 o;
    o.x = pk2(acc[8 * q + 0], acc[8 * q + 1]); o.y = pk2(acc[8 * q + 2], acc[8 * q + 3]);
    o.z = pk2(acc[8 * q + 4], acc[8 * q + 5]); o.w = pk2(acc[8 * q + 6], acc[8 * q + 7]);
    st_sc1(cx.ws, dst + (2 * q + hi) * 512 + (l & 31) * 16, o);
  }
}
__device__ __forceinline__ void mod_part(const Params& p, const Ctx& cx_, LAS unsigned char* L, int t32);
__device__ __forceinline__ void proj_role(const Params& p, const Ctx& cx, LAS unsigned char* L, int h, int kind, int j) {
  const int w = cx.wave, l = cx.lane;
  {
    u32x4 A[32];
    if (kind == 2) ln_part<2>(p, cx, 64 * h + 48, 3 * h + 2);
    else ln_part<3>(p, cx, 64 * h + 24 * kind, 3 * h + kind);
    TRG(128 + j, 2);
    stage_w(kind == 0 ? p.Wq : (kind == 1 ? p.Wk : p.Wv), h, cx, L);
    xn_ready(cx);
    const u16* xn = (const u16*)(p.ws + OFF_XN);
    u32x4 x0[8];
#pragma unroll
    for (int i = 0; i < 8; ++i) x0[i] = *(const GAS u32x4*)(xn + (size_t)(8 * w + i) * DM + l * 8);
    asm volatile("s_waitcnt vmcnt(8)" ::: "memory");
    BAR();
    read_wt(cx, L, w & 3, A);
    BAR();
    TRG(128 + j, 4);
    const size_t obase = kind == 0 ? OFF_QX : (kind == 1 ? OFF_KX : OFF_VX);
    auto dma = [&](int T, int buf) {
#pragma unroll
      for (int i = 0; i < 8; ++i) GLDS(xn + (size_t)(64 * T + 8 * w + i) * DM + l * 8, L + buf * (64 * XSTR) + (8 * w + i) * XSTR);
    };
#pragma unroll
    for (int i = 0; i < 8; ++i) *(LAS u32x4*)(L + (8 * w + i) * XSTR + l * 16) = x0[i];
    BAR();
#pragma unroll 1
    for (int T = 0; T < 32; ++T) {
      if (T + 1 < 32) dma(T + 1, (T + 1) & 1);
      const int s = 2 * T + (w >> 2);
      proj_tile(cx, L + (T & 1) * (64 * XSTR) + (w >> 2) * (32 * XSTR), A, (unsigned)(obase + ((size_t)s * NH + h) * 8192) + (w & 3) * 2048);
      DRAIN();
      BAR();
      if (w == 0 && l == 0) {
        if (kind == 2) __hip_atomic_store(cx.ctrl + C_V + h * 32, (unsigned)T, RLX_AGENT);
        else arrive(cx, C_KQ + T * 32);
      }
      if (T == 0) TRG(128 + j, 3);
    }
  }
  if (kind == 2) mod_part(p, cx, L, h);
}
constexpr int U_SPLIT = 3;
__device__ __forceinline__ void u_aload(const Ctx& cx, int h, int seg, u32x4 (&A)[8]) {
  const int l = cx.lane;
#pragma unroll
  for (int u = 0; u < 8; ++u) A[u] = ld_sc1(cx.ws, (unsigned)(OFF_VX + ((size_t)seg * NH + h) * 8192) + (2 * u + (l >> 5)) * 512 + (l & 31) * 16);
}
__device__ __forceinline__ void u_seg(const Ctx& cx, int h, int seg, const u32x4 (&A)[8], const u32x4 (&B)[2][8]) {
  const int w = cx.wave, l = cx.lane, hi = l >> 5;
#pragma unroll
  for (int nt = 0; nt < 2; ++nt) {
    f32x16 acc = {0.f};
#pragma unroll
    for (int u = 0; u < 8; ++u) acc = mfma32(A[u], B[nt][u], acc);
    const int col = 64 * w + 32 * nt + (l & 31);
    const unsigned base = (unsigned)(OFF_U + ((size_t)seg * NCOL + (col >> 3)) * U_SC) + ((h * 5 + hi) * 8 + (col & 7)) * 16;
    u32x4 o;
    o.x = pk2(acc[0], acc[1]); o.y = pk2(acc[2], acc[3]); o.z = pk2(acc[8], acc[9]); o.w = pk2(acc[10], acc[11]);
    st_sc1(cx.ws, base, o);
    o.x = pk2(acc[4], acc[5]); o.y = pk2(acc[6], acc[7]); o.z = pk2(acc[12], acc[13]); o.w = pk2(acc[14], acc[15]);
    st_sc1(cx.ws, base + 256, o);
    __builtin_amdgcn_sched_barrier(0);
  }
}
__device__ __forceinline__ void u_early(const Ctx& cx, int h, int k, const u32x4 (&B)[2][8]) {
#pragma unroll 1
  for (int T = 0; T < U_SPLIT; ++T) {
    wait_words(cx, C_V + h * 32, 1, (unsigned)(T + 1));
    u32x4 A[8];
    u_aload(cx, h, 2 * T + k, A);
    u_seg(cx, h, 2 * T + k, A, B);
    DRAIN();
    BAR();
    if (cx.wave == 0 && cx.lane == 0) arrive(cx, C_U + (2 * T + k) * 32);
  }
}
__device__ __forceinline__ void u_role(const Params& p, const Ctx& cx_, LAS unsigned char* L, int h, int j) {
  u32x4 B[2][8];
  wo_issue(p, cx_, L, h);
  DRAIN();
  BAR();
  { const Ctx cx = fresh(cx_); TRG(128 + j, 2); }
  wvo_part(p, cx_, L, h, B);
  const Ctx cx = fresh(cx_);
  const int w = cx.wave, l = cx.lane;
  u_early(cx, h, 0, B);
  TRG(128 + j, 3);
  auto tile = [&](const u32x4 (&A)[2][8], u32x4 (&An)[2][8], int T) {
    TRU(0);
    const bool pre = (T + 1 < 32) && test_word(cx, C_V + h * 32, (unsigned)(T + 2));
    if (pre) { u_aload(cx, h, 2 * T + 2, An[0]); u_aload(cx, h, 2 * T + 3, An[1]); }
    u_seg(cx, h, 2 * T, A[0], B);
    u_seg(cx, h, 2 * T + 1, A[1], B);
    TRU(2);
    DRAIN();
    BAR();
    TRU(3);
    if (w == 0 && l < 2) arrive(cx, C_U + (2 * T + l) * 32);
    if (!pre && T + 1 < 32) { wait_words(cx, C_V + h * 32, 1, (unsigned)(T + 2)); u_aload(cx, h, 2 * T + 2, An[0]); u_aload(cx, h, 2 * T + 3, An[1]); }
  };
  u32x4 Aa[2][8], Ab[2][8];
  wait_words(cx, C_V + h * 32, 1, (unsigned)(U_SPLIT + 1));
  u_aload(cx, h, 2 * U_SPLIT, Aa[0]); u_aload(cx, h, 2 * U_SPLIT + 1, Aa[1]);
  tile(Aa, Ab, U_SPLIT);
#pragma unroll 1
  for (int T = U_SPLIT + 1; T < 32; T += 2) {
    tile(Ab, Aa, T);
    tile(Aa, Ab, T + 1);
  }
}

__device__ __forceinline__ void topk_part(const Ctx& cx, LAS int* IDX) {
  const int w = cx.wave, l = cx.lane, tid = w * 64 + l;
  LAS unsigned* CAND = (LAS unsigned*)(IDX + 16);
  unsigned key[4];
#pragma unroll
  for (int i = 0; i < 4; ++i) {
    const int row = tid + 512 * i;
    const unsigned rb = __builtin_amdgcn_raw_buffer_load_b32(cx.ws, (unsigned)(OFF_STAT + (size_t)row * 16 + 8), 0, AUX_SC1);
    const unsigned mono = (rb & 0x80000000u) ? ~rb : (rb | 0x80000000u);
    key[i] = (mono & 0xFFFF0000u) | (unsigned)(0xFFFF - row);
  }
  unsigned mine = 0;
#pragma unroll
  for (int c = 0; c < TOPC; ++c) {
    const unsigned b = wave_umax(max(max(key[0], key[1]), max(key[2], key[3])));
#pragma unroll
    for (int i = 0; i < 4; ++i) if (key[i] == b) key[i] = 0u;
    if (l == c) mine = b;
  }
  if (l < 8) CAND[w * 8 + l] = mine;
  BAR();
  unsigned k2 = CAND[l];
#pragma unroll
  for (int c = 0; c < TOPC; ++c) {
    const unsigned b = wave_umax(k2);
    if (k2 == b) k2 = 0u;
    if (l == c) mine = b;
  }
  if (w == 0 && l < 8) {
    const unsigned mono = mine & 0xFFFF0000u;
    const float r = u2f((mono & 0x80000000u) ? (mono & 0x7fffffffu) : (~mono & 0xFFFF0000u));
    IDX[l] = 0xFFFF - (int)(mine & 0xFFFFu);
    IDX[8 + l] = (int)f2u(bfr(1.f / (1.f + __expf(-r))));
  }
  BAR();
}

__device__ __forceinline__ void mod_part(const Params& p, const Ctx& cx_, LAS unsigned char* L, int t32) {
  const Ctx cx = fresh(cx_);
  const int w = cx.wave, l = cx.lane, tid = w * 64 + l, g = l >> 4, n = l & 15;
  LAS int* IDX = (LAS int*)(L + 140 * 1024);
  LAS float* UX = (LAS float*)(L + 141 * 1024);
  if (t32 < 28) {
    const u16* wsrc = (w < 4 ? p.w_gate : p.w_up) + 64 * t32 + (l & 7) * 8;
#pragma unroll
    for (int i = 0; i < 16; ++i) {
      const int blk = (w & 3) * 16 + i;
      GLDS(wsrc + (size_t)(8 * blk + (l >> 3)) * FF, L + (w >> 2) * 65536 + blk * 1024);
    }
  }
  topk_part(cx, IDX);
  DRAIN();
  BAR();
  const int q4 = n >> 2, pp = l & 3;
  if (t32 < 28) {
    const unsigned xb = (unsigned)(OFF_XN + (size_t)IDX[l & 7] * 1024) + g * 16;
    u32x4 A[16];
#pragma unroll
    for (int u = 0; u < 16; ++u) A[u] = ld_sc1(cx.ws, xb + u * 64);
    LAS unsigned char* b0 = L + (w >> 2) * 65536 + (8 * g + q4) * 128 + (w & 3) * 32 + pp * 8;
    f32x4 acc = {0.f, 0.f, 0.f, 0.f};
#pragma unroll
    for (int u = 0; u < 16; ++u) {
      const u32x2 lo = tr_read(b0 + u * 4096), h2 = tr_read(b0 + u * 4096 + 512);
      acc = mfma16(A[u], (u32x4){lo.x, lo.y, h2.x, h2.y}, acc);
    }
    if (w >= 4 && g < 2) {
#pragma unroll
      for (int r = 0; r < 4; ++r) UX[(4 * g + r) * 64 + (w & 3) * 16 + n] = bfr(acc[r]);
    }
    BAR();
    LAS u16* GT = (LAS u16*)(L + 144 * 1024);
    if (w < 4 && g < 2) {
#pragma unroll
      for (int r = 0; r < 4; ++r) {
        const float gs = bfr(acc[r]);
        const float sg = bfr(gs / (1.f + __expf(-gs)));
        GT[(4 * g + r) * 64 + w * 16 + n] = f2bf(sg * UX[(4 * g + r) * 64 + w * 16 + n]);
      }
    }
    BAR();
    if (w == 0) {
      st_sc1(cx.ws, (unsigned)(OFF_GU + (size_t)(l >> 3) * (FF * 2) + t32 * 128 + (l & 7) * 16), *(LAS u32x4*)((LAS unsigned char*)GT + l * 16));
      DRAIN();
      if (l == 0) arrive(cx, C_GU);
    }
  }
  {
    const u16* wsrc = p.w_down + 16 * t32 + (l & 1) * 8;
#pragma unroll
    for (int i = 0; i < 7; ++i) {
      const int blk = w * 7 + i;
      GLDS(wsrc + (size_t)(32 * blk + (l >> 1)) * DM, L + blk * 1024);
    }
    if (w == 0) wait_words(cx, C_GU, 1, 28u);
    DRAIN();
    BAR();
    const unsigned gb = (unsigned)(OFF_GU + (size_t)(l & 7) * (FF * 2)) + (7 * w) * 64 + g * 16;
    u32x4 A[7];
#pragma unroll
    for (int u = 0; u < 7; ++u) A[u] = ld_sc1(cx.ws, gb + u * 64);
    LAS unsigned char* b0 = L + (7 * w) * 1024 + (8 * g + q4) * 32 + pp * 8;
    f32x4 acc = {0.f, 0.f, 0.f, 0.f};
#pragma unroll
    for (int u = 0; u < 7; ++u) {
      const u32x2 lo = tr_read(b0 + u * 1024), h2 = tr_read(b0 + u * 1024 + 128);
      acc = mfma16(A[u], (u32x4){lo.x, lo.y, h2.x, h2.y}, acc);
    }
    LAS float* PR = (LAS float*)(L + 65536);
    if (g < 2) {
#pragma unroll
      for (int r = 0; r < 4; ++r) PR[(w * 8 + 4 * g + r) * 16 + n] = acc[r];
    }
    BAR();
    if (w < 2) {
      const int r = tid >> 4;
      float y = 0.f;
#pragma unroll
      for (int k = 0; k < 8; ++k) y += PR[(k * 8 + r) * 16 + n];
      st_sc1_32(cx.ws, (unsigned)(OFF_Y + (size_t)(r * DM + 16 * t32 + n) * 4), f2u(bfr(bfr(y) * u2f((unsigned)IDX[8 + r]))));
      DRAIN();
    }
    BAR();
    if (tid == 0) arrive(cx, C_Y);
  }
}

__device__ __forceinline__ void to_role(const Params& p, const Ctx& cx_, LAS unsigned char* L, int x, int f) {
  const int t32 = x * 8 + f;
  {
    u32x4 B[2][8];
    wo_issue(p, cx_, L, 32 + t32);
    DRAIN();
    BAR();
    wvo_part(p, cx_, L, 32 + t32, B);
    u_early(fresh(cx_), t32, 1, B);
  }
  const Ctx cx = fresh(cx_);
  const int w = cx.wave, l = cx.lane, tid = w * 64 + l, hi = l >> 5;
  TRG(96 + t32, 2);
  LAS int* IDX = (LAS int*)(L + 140 * 1024);
  LAS f32x4* SELT = (LAS f32x4*)(L + 141 * 1024);
  if (w == 0) { wait_words(cx, C_XN, 8, 12u); wait_words(cx, C_RDY, 8, 12u); }
  BAR();
  const unsigned pol = (unsigned)__builtin_amdgcn_readfirstlane(__hip_atomic_load(cx.ctrl + C_POL, RLX_AGENT)) & 1u;
  topk_part(cx, IDX);
  TRG(96 + t32, 3);
  const int hf = f & 1;
  const int cb0 = x * 16 + hf * 8;
  const int ocol = 128 * x + 64 * hf + (tid & 15) * 4;
  LAS float* RD = (LAS float*)L;
#pragma unroll 1
  for (int s = f >> 1; s < NSEG; s += 4) {
    TRT(0);
    const bool last = s + 4 >= NSEG;
    if (w == 0) { wait_words(cx, C_U + s * 32, 1, 32u); wait_words(cx, C_KQ + (s >> 1) * 32, 1, 64u); if (last) wait_words(cx, C_Y, 1, 32u); }
    BAR();
    TRT(1);
    if (last && tid < 128) {
      const int sl = tid >> 4, row = IDX[sl];
      if (((row >> 5) & 3) == (f >> 1) && (row >> 5) < s) {
        const f32x4 y4 = __builtin_bit_cast(f32x4, ld_sc1(cx.ws, (unsigned)(OFF_Y + (size_t)(sl * DM + ocol) * 4)));
        const f32x4 t1 = SELT[tid * 2], xn4 = SELT[tid * 2 + 1];
        u32x2 ov; ov.x = pk2(t1[0] + bfr(xn4[0] + y4[0]), t1[1] + bfr(xn4[1] + y4[1])); ov.y = pk2(t1[2] + bfr(xn4[2] + y4[2]), t1[3] + bfr(xn4[3] + y4[3]));
        *(GAS u32x2*)(p.out + (size_t)row * DM + ocol) = ov;
      }
    }
    const unsigned ub = (unsigned)(OFF_U + ((size_t)s * NCOL + cb0 + ((l & 31) >> 3)) * U_SC) + (l & 7) * 16 + w * (4 * 5 * 128);
    u32x4 Bx[2][8];
#pragma unroll
    for (int nt = 0; nt < 2; ++nt)
#pragma unroll
      for (int u = 0; u < 8; ++u) Bx[nt][u] = ld_sc1(cx.ws, ub + nt * (4 * (unsigned)U_SC) + ((u >> 1) * 5 + 2 * (u & 1) + hi) * 128);
    {
      u32x4 t[8];
      const unsigned kb = (unsigned)(((size_t)s * NH + x * 8 + hf * 4) * 8192) + tid * 16;
#pragma unroll
      for (int i = 0; i < 4; ++i) { t[i] = ld_sc1(cx.ws, (unsigned)OFF_KX + kb + i * 8192); t[4 + i] = ld_sc1(cx.ws, (unsigned)OFF_QX + kb + i * 8192); }
#pragma unroll
      for (int i = 0; i < 8; ++i) asm volatile("" ::"v"(t[i]));
    }
    TRT(2);
    const int orow = 32 * s + (tid >> 4);
    const u32x2 xin = *(const GAS u32x2*)(p.x + (size_t)orow * DM + ocol);
    const u32x2 stat = __builtin_amdgcn_raw_buffer_load_b64(cx.ws, (unsigned)(OFF_STAT + (size_t)orow * 16), 0, AUX_SC1);
    int sel = -1;
#pragma unroll
    for (int c = 0; c < TOPC; ++c) if (IDX[c] == orow) sel = c;
    f32x4 y4 = {0.f, 0.f, 0.f, 0.f};
    if (last && sel >= 0) y4 = __builtin_bit_cast(f32x4, ld_sc1(cx.ws, (unsigned)(OFF_Y + (size_t)(sel * DM + ocol) * 4)));
    if (w == 0) {
      const unsigned fa = ex_addr(l < 32 ? FPU + l * 128 : FUMU + (cb0 + (l & 7)) * 128, pol);
      for (unsigned spins = 0;; ++spins) {
        asm volatile("" ::: "memory");
        const unsigned v = __builtin_amdgcn_raw_buffer_load_b32(cx.ex, fa, 0, AUX_SC1) + 1u;
        if (__all(v >= (unsigned)(s + 1))) break;
        if (spins > WAIT_CAP) { __hip_atomic_store(cx.ctrl + C_TMO, 1u, RLX_AGENT); break; }
        if ((spins & 63u) == 63u && __builtin_amdgcn_readfirstlane(__hip_atomic_load(cx.ctrl + C_TMO, RLX_AGENT)) == 1u) break;
        __builtin_amdgcn_s_sleep(TO_SLEEP);
      }
    }
    BAR();
    TRT(3);
    const unsigned pb = (unsigned)(OFF_PX + ((size_t)s * NH + w * 4) * 2560) + (l & 31) * 16;
    u32x4 Ax[8], Am[2], Bm[2][2];
#pragma unroll
    for (int u = 0; u < 8; ++u) Ax[u] = ld_sc1(cx.ws, pb + ((u >> 1) * 5 + 2 * (u & 1) + hi) * 512);
#pragma unroll
    for (int v = 0; v < 2; ++v) {
      Am[v] = ld_sc1(cx.ws, pb + ((2 * v + hi) * 5 + 4) * 512);
      Bm[0][v] = ld_sc1(cx.ws, ub + ((2 * v + hi) * 5 + 4) * 128);
      Bm[1][v] = ld_sc1(cx.ws, ub + 4 * (unsigned)U_SC + ((2 * v + hi) * 5 + 4) * 128);
    }
#pragma unroll
    for (int nt = 0; nt < 2; ++nt) {
      f32x16 acc = {0.f};
#pragma unroll
      for (int u = 0; u < 8; ++u) acc = mfma32(Ax[u], Bx[nt][u], acc);
#pragma unroll
      for (int v = 0; v < 2; ++v) acc = mfma32(Am[v], Bm[nt][v], acc);
#pragma unroll
      for (int r = 0; r < 16; ++r) RD[(w * 32 + (r & 3) + 8 * (r >> 2) + 4 * hi) * 68 + nt * 32 + (l & 31)] = acc[r];
    }
    BAR();
    TRT(4);
    {
      LAS float* q = RD + (tid >> 4) * 68 + (tid & 15) * 4;
      f32x4 sum = *(LAS f32x4*)q;
#pragma unroll
      for (int k = 1; k < 8; ++k) sum += *(LAS f32x4*)(q + k * 32 * 68);
      const float mb = u2f(stat.x), den = u2f(stat.y);
      f32x4 t1, xn4;
#pragma unroll
      for (int k = 0; k < 4; ++k) {
        const float xv = (k & 1) ? bfhi(xin[k >> 1]) : bflo(xin[k >> 1]);
        xn4[k] = bfr(bfr(xv - mb) / den);
        t1[k] = bfr(xv + bfr(sum[k]));
      }
      if (sel >= 0 && !last) { SELT[(sel * 16 + (tid & 15)) * 2] = t1; SELT[(sel * 16 + (tid & 15)) * 2 + 1] = xn4; }
      else {
        u32x2 ov; ov.x = pk2(t1[0] + bfr(xn4[0] + y4[0]), t1[1] + bfr(xn4[1] + y4[1])); ov.y = pk2(t1[2] + bfr(xn4[2] + y4[2]), t1[3] + bfr(xn4[3] + y4[3]));
        *(GAS u32x2*)(p.out + (size_t)orow * DM + ocol) = ov;
      }
    }
  }
}

constexpr int LDS_BYTES = 160 * 1024;

__global__ void __launch_bounds__(512, 2) k_fused(Params p) {
  extern __shared__ __attribute__((aligned(16))) unsigned char lds_raw[];
  LAS unsigned char* L = (LAS unsigned char*)lds_raw;
  const int tid = threadIdx.x;
  Ctx cx;
  cx.wave = __builtin_amdgcn_readfirstlane(tid >> 6); cx.lane = tid & 63;
  cx.ctrl = (gu32*)p.ws;
  const uintptr_t exb = ((uintptr_t)p.ws + CTRL_BYTES + 16383) & ~(uintptr_t)16383;
  cx.ex = __builtin_amdgcn_make_buffer_rsrc((void*)exb, 0, (int)EX_BYTES, 0x00020000);
  cx.exs = (u32x4){(unsigned)exb, (unsigned)(exb >> 32) & 0xffffu, (unsigned)EX_BYTES, 0x00020000u};
  cx.ws = __builtin_amdgcn_make_buffer_rsrc((void*)p.ws, 0, (int)WS_END, 0x00020000);
  cx.pol = 0;
  volatile LAS int* sh = (volatile LAS int*)(L + 155 * 1024);
  if (tid == 0) {
    const unsigned x = xcc_id() & 7u;
    const unsigned t = __hip_atomic_fetch_add(cx.ctrl + C_RC + x * 32, 1u, RLX_AGENT) + 1u;
    __hip_atomic_fetch_add(cx.ctrl + C_TOT, 1u, RLX_AGENT);
    int role;
    if (t < 32u) role = (int)(x * 32 + t);
    else {
      const unsigned o = __hip_atomic_fetch_add(cx.ctrl + C_OVF, 1u, RLX_AGENT) + 1u;
      unsigned spins = 0; role = -1;
      while (__hip_atomic_load(cx.ctrl + C_TOT, RLX_AGENT) + 1u < 256u) { if (++spins > WAIT_CAP) break; __builtin_amdgcn_s_sleep(2); }
      unsigned seen = 0;
      for (int xx = 0; xx < 8 && role < 0; ++xx) {
        const unsigned cnt = __hip_atomic_load(cx.ctrl + C_RC + xx * 32, RLX_AGENT) + 1u;
        const unsigned holes = cnt < 32u ? 32u - cnt : 0u;
        if (o < seen + holes) role = xx * 32 + (int)(cnt + (o - seen));
        seen += holes;
      }
    }
    sh[0] = role;
  }
  __syncthreads();
  const int role = __builtin_amdgcn_readfirstlane(sh[0]);
  __syncthreads();
  if (role < 0) return;
  TRG(role < 128 ? ((role & 31) < 8 ? (role >> 5) * 8 + (role & 31) : ((role & 31) < 24 ? 32 + (role >> 5) * 16 + (role & 31) - 8 : 96 + (role >> 5) * 8 + (role & 31) - 24)) : role, 0);
  const int rx = role >> 5, rt_ = role & 31;
  if (rx >= 4) {
    const int j = role - 128, h = j >> 2, k = j & 3;
    if (k < 3) proj_role(p, cx, L, h, k, j);
    else u_role(p, cx, L, h, j);
    TRG(role, 1);
    return;
  }
  if (rt_ >= 24) { to_role(p, cx, L, rx, rt_ - 24); TRG(96 + rx * 8 + rt_ - 24, 1); return; }
  if (role == 0 && cx.wave == 0) {
    unsigned best0 = ~0u, best1 = ~0u;
    for (int rep = 0; rep < 3; ++rep) {
      unsigned t0 = now32();
      if (cx.lane == 0) st_sc1_32(cx.ex, TAGU * 2, 0u);
      DRAIN();
      unsigned t1 = now32();
      best0 = min(best0, t1 - t0);
      if (cx.lane == 0) st_sc1_32(cx.ex, TAGU * 2 + 8192u, 0u);
      DRAIN();
      t0 = now32();
      best1 = min(best1, t0 - t1);
    }
    if (cx.lane == 0) __hip_atomic_store(cx.ctrl + C_POL, best1 < best0 ? 1u : 0u, RLX_AGENT);
  }
  if (role == 0) __syncthreads();
  if (rt_ < 8) head_role(p, cx, L, rx * 8 + rt_);
  else col_role(p, cx, L, rx * 16 + rt_ - 8);
}

extern "C" void kernel_launch(void* const* d_in, const int* in_sizes, int n_in, void* d_out, int out_size, void* d_ws, size_t ws_size, hipStream_t stream) {
  Params p;
  p.x = (const u16*)d_in[0]; p.mem0 = (const u16*)d_in[1]; p.Wq = (const u16*)d_in[2]; p.Wk = (const u16*)d_in[3]; p.Wv = (const u16*)d_in[4]; p.Wo = (const u16*)d_in[5];
  p.w_r = (const u16*)d_in[8]; p.w_gate = (const u16*)d_in[9]; p.w_up = (const u16*)d_in[10]; p.w_down = (const u16*)d_in[11];
  p.out = (u16*)d_out; p.ws = (unsigned char*)d_ws;
  p.dh = D_H; p.dm = D_M; p.dp = D_P; p.pad = 0;
  (void)hipFuncSetAttribute((const void*)k_fused, hipFuncAttributeMaxDynamicSharedMemorySize, LDS_BYTES);
  (void)hipMemsetAsync(d_ws, 0xFF, CTRL_BYTES, stream);
  k_fused<<<256, 512, LDS_BYTES, stream>>>(p);
}
```

```cpp
#include <hip/hip_runtime.h>
#include <hip/hip_bf16.h>
#include <stdint.h>

#define GAS __attribute__((address_space(1)))
#define LAS __attribute__((address_space(3)))
typedef unsigned short u16;
typedef __bf16 bf16x8 __attribute__((ext_vector_type(8)));
typedef __bf16 bf16x16 __attribute__((ext_vector_type(16)));
typedef __bf16 bf16x2 __attribute__((ext_vector_type(2)));
typedef float f32x4 __attribute__((ext_vector_type(4)));
typedef float f32x2 __attribute__((ext_vector_type(2)));
typedef float f32x16 __attribute__((ext_vector_type(16)));
typedef unsigned u32x4 __attribute__((ext_vector_type(4)));
typedef unsigned u32x2 __attribute__((ext_vector_type(2)));
typedef unsigned u32x8 __attribute__((ext_vector_type(8)));
typedef GAS unsigned gu32;
#define RLX_AGENT __ATOMIC_RELAXED, __HIP_MEMORY_SCOPE_AGENT

constexpr int S_ = 2048, DM = 512, NH = 32, DH = 128, HD = 4096, NSEG = 64, FF = 1792, TOPC = 8;
constexpr int NCOL = 64;

constexpr size_t CTRL_BYTES = 32768;
constexpr int RM = 4, RP = 3;
constexpr unsigned PBASE = 32768u, PSLOT = 20480u, FPU = PBASE + 4 * PSLOT, FUMU = FPU + 8192u;
constexpr size_t EX_BYTES = 1u << 20;
static_assert(RM <= 4 && RP <= 4 && FUMU + 8192u <= 131072u, "exchange layout");
constexpr size_t OFF_XN = 4u << 20;
constexpr size_t OFF_KX = OFF_XN + (size_t)S_ * DM * 2;
constexpr size_t OFF_QX = OFF_KX + (size_t)S_ * HD * 2;
constexpr size_t OFF_VX = OFF_QX + (size_t)S_ * HD * 2;
constexpr size_t OFF_U = OFF_VX + (size_t)S_ * HD * 2;
constexpr size_t U_SC = 160 * 128;
constexpr size_t OFF_WVO = OFF_U + (size_t)NSEG * NCOL * U_SC;
constexpr size_t OFF_PX = OFF_WVO + (size_t)NCOL * 16 * 8 * 64 * 32;
constexpr size_t OFF_STAT = OFF_PX + (size_t)NSEG * NH * 5 * 512;
constexpr size_t OFF_GU = OFF_STAT + (size_t)S_ * 16;
constexpr size_t OFF_Y = OFF_GU + TOPC * FF * 4;
constexpr size_t OFF_DBG = OFF_Y + TOPC * DM * 4;
constexpr size_t WS_END = OFF_DBG + (1u << 20);

constexpr int C_RC = 0, C_OVF = 256, C_TOT = 288, C_TMO = 352, C_GU = 384, C_Y = 416, C_POL = 1280;
constexpr int C_XN = 512, C_WVO = 768, C_RDY = 1024;
constexpr int C_V = 2048;
constexpr int C_KQ = 4096, C_U = 6144;

#define D_H 3400
#define D_M 600
#define D_P 1500

#define TO_SLEEP 8
struct Params {
  const u16 *x, *mem0, *Wq, *Wk, *Wv, *Wo, *w_r, *w_gate, *w_up, *w_down;
  u16* out;
  unsigned char* ws;
  int dh, dm, dp, pad;
};

__device__ __forceinline__ unsigned f2u(float f) { return __builtin_bit_cast(unsigned, f); }
__device__ __forceinline__ float u2f(unsigned u) { return __builtin_bit_cast(float, u); }
__device__ __forceinline__ float bf2f(u16 b) { return u2f((unsigned)b << 16); }
__device__ __forceinline__ float bflo(unsigned u) { return u2f(u << 16); }
__device__ __forceinline__ float bfhi(unsigned u) { return u2f(u & 0xffff0000u); }
__device__ __forceinline__ u16 f2bf(float f) { __bf16 b = (__bf16)f; return __builtin_bit_cast(u16, b); }
__device__ __forceinline__ float bfr(float f) { return bf2f(f2bf(f)); }
__device__ __forceinline__ unsigned pk2(float lo, float hi) { f32x2 v = {lo, hi}; bf16x2 b = __builtin_convertvector(v, bf16x2); return __builtin_bit_cast(unsigned, b); }

constexpr int AUX_SC1 = 16;
constexpr unsigned SENT = 0xFFFFFFFFu;
constexpr unsigned SPIN_CAP = 1u << 15;
constexpr unsigned WAIT_CAP = 1u << 17;
__device__ __forceinline__ unsigned xcc_id() { return (unsigned)__builtin_amdgcn_s_getreg(20 | (3 << 11)) & 0xfu; }
__device__ __forceinline__ unsigned now32() { unsigned long long t; asm volatile("s_memtime %0\n\ts_waitcnt lgkmcnt(0)" : "=s"(t)::"memory"); return (unsigned)t; }
__device__ __forceinline__ u32x4 ld_sc1(__amdgpu_buffer_rsrc_t rs, unsigned off) { return __builtin_amdgcn_raw_buffer_load_b128(rs, off, 0, AUX_SC1); }
__device__ __forceinline__ void st_sc1(__amdgpu_buffer_rsrc_t rs, unsigned off, u32x4 v) { __builtin_amdgcn_raw_buffer_store_b128(v, rs, off, 0, AUX_SC1); }
__device__ __forceinline__ void st_sc1s(__amdgpu_buffer_rsrc_t rs, unsigned off, unsigned soff, u32x4 v) { __builtin_amdgcn_raw_buffer_store_b128(v, rs, off, soff, AUX_SC1); }
__device__ __forceinline__ void st_sc1_64(__amdgpu_buffer_rsrc_t rs, unsigned off, u32x2 v) { __builtin_amdgcn_raw_buffer_store_b64(v, rs, off, 0, AUX_SC1); }
__device__ __forceinline__ void st_sc1_32(__amdgpu_buffer_rsrc_t rs, unsigned off, unsigned v) { __builtin_amdgcn_raw_buffer_store_b32(v, rs, off, 0, AUX_SC1); }
__device__ __forceinline__ unsigned nosent(unsigned x) { return min(x, 0xFFFFFFFEu); }
__device__ __forceinline__ f32x4 mfma16(u32x4 a, u32x4 b, f32x4 c) { return __builtin_amdgcn_mfma_f32_16x16x32_bf16(__builtin_bit_cast(bf16x8, a), __builtin_bit_cast(bf16x8, b), c, 0, 0, 0); }
__device__ __forceinline__ f32x16 mfma32(u32x4 a, u32x4 b, f32x16 c) { return __builtin_amdgcn_mfma_f32_32x32x16_bf16(__builtin_bit_cast(bf16x8, a), __builtin_bit_cast(bf16x8, b), c, 0, 0, 0); }
__device__ __forceinline__ f32x4 smfma(u32x4 a, u32x8 b, f32x4 c, int idx) { return __builtin_amdgcn_smfmac_f32_16x16x64_bf16(__builtin_bit_cast(bf16x8, a), __builtin_bit_cast(bf16x16, b), c, idx, 0, 0); }
typedef short v4i16_t __attribute__((ext_vector_type(4)));
__device__ __forceinline__ u32x2 tr_read(LAS unsigned char* p) { return __builtin_bit_cast(u32x2, __builtin_amdgcn_ds_read_tr16_b64_v4i16((LAS v4i16_t*)p)); }
__device__ __forceinline__ unsigned ex_addr(unsigned i, unsigned pol) { return (i >> 13) * 32768u + (i & 8191u) + (pol & 1u) * 8192u; }
__device__ __forceinline__ unsigned rep_off(unsigned r, unsigned pol) { return (r >> 1) * 524288u + ((((r + 1) >> 1) ^ (pol >> 1)) & 1u) * 16384u; }
__device__ __forceinline__ unsigned dpp_ror8(unsigned x) { return (unsigned)__builtin_amdgcn_update_dpp(0, (int)x, 0x128, 0xf, 0xf, true); }
#define BAR() do { asm volatile("" ::: "memory"); __builtin_amdgcn_s_waitcnt(0xC07F); __builtin_amdgcn_s_barrier(); asm volatile("" ::: "memory"); } while (0)
#define DRAIN() do { asm volatile("s_waitcnt vmcnt(0)" ::: "memory"); __builtin_amdgcn_s_waitcnt(0x0F70); } while (0)
#define GLDS(src, dst) __builtin_amdgcn_global_load_lds((const GAS unsigned*)(src), (LAS unsigned*)(dst), 16, 0, 0)

struct Ctx {
  __amdgpu_buffer_rsrc_t ex;
  u32x4 exs;
  __amdgpu_buffer_rsrc_t ws;
  gu32* ctrl;
  unsigned pol;
  int wave, lane;
};

__device__ __forceinline__ int lane_id() { int l; asm volatile("v_mbcnt_lo_u32_b32 %0, -1, 0\n\tv_mbcnt_hi_u32_b32 %0, -1, %0" : "=v"(l)); return l; }
__device__ __forceinline__ Ctx fresh(const Ctx& c) { Ctx r = c; r.lane = lane_id(); return r; }
__device__ __forceinline__ void arrive(const Ctx& cx, int word) { __hip_atomic_fetch_add(cx.ctrl + word, 1u, RLX_AGENT); }
__device__ __forceinline__ void wait_words(const Ctx& cx, int word, int n, unsigned want) {
  gu32* a = cx.ctrl + word + (cx.lane < n ? cx.lane : 0) * 32;
  for (unsigned spins = 0;; ++spins) {
    const unsigned v = __hip_atomic_load(a, RLX_AGENT) + 1u;
    if (__all(v >= want)) break;
    if (spins > WAIT_CAP) { __hip_atomic_store(cx.ctrl + C_TMO, 1u, RLX_AGENT); break; }
    if ((spins & 255u) == 255u && __builtin_amdgcn_readfirstlane(__hip_atomic_load(cx.ctrl + C_TMO, RLX_AGENT)) == 1u) break;
    __builtin_amdgcn_s_sleep(2);
  }
  __builtin_amdgcn_fence(__ATOMIC_ACQUIRE, "wavefront");
}
#define AD_UP 24
#define AD_DN 0
__device__ __forceinline__ int adapt(int d, int d0, unsigned miss, int s) {
  if (miss && s >= 4) d = min(d + AD_UP, d0 + 500);
  if ((s & 7) == 7) d -= AD_DN;
  return d;
}
__device__ __forceinline__ bool test_word(const Ctx& cx, int word, unsigned want) {
  const unsigned v = (unsigned)__builtin_amdgcn_readfirstlane(__hip_atomic_load(cx.ctrl + word, RLX_AGENT)) + 1u;
  __builtin_amdgcn_fence(__ATOMIC_ACQUIRE, "wavefront");
  return v >= want;
}
__device__ __forceinline__ unsigned get_pol(const Ctx& cx) {
  wait_words(cx, C_POL, 4, 1u);
  unsigned best = 0, bv = ~0u;
  unsigned m[4];
#pragma unroll
  for (int x = 0; x < 4; ++x) m[x] = (unsigned)__builtin_amdgcn_readfirstlane(__hip_atomic_load(cx.ctrl + C_POL + x * 32, RLX_AGENT));
#pragma unroll
  for (int c = 0; c < 4; ++c) {
    unsigned mx = 0, sum = 0;
#pragma unroll
    for (int x = 0; x < 4; ++x) { const unsigned v = (m[x] >> (8 * c)) & 127u; mx = max(mx, v); sum += v; }
    const unsigned sc = mx * 4 + sum;
    if (sc < bv) { bv = sc; best = (unsigned)c; }
  }
  return best;
}
__device__ __forceinline__ void timed_wait(unsigned tref, int d) {
  while ((int)(now32() - tref) < d - 200) __builtin_amdgcn_s_sleep(1);
  while ((int)(now32() - tref) < d) {}
}

template <class HOOK>
__device__ __forceinline__ u32x2 front(const Params& p, const Ctx& cx, LAS unsigned char* MEMIMG, const u32x8 (&W)[2][8], int s, unsigned& miss, unsigned& tmid, HOOK hook) {
  const int w = cx.wave, l = cx.lane;
  u32x4 v;
  if (s == 0) {
    const u16* mp = p.mem0 + (l & 7) * DM + (8 * w + (l >> 3)) * 8;
    asm volatile("global_load_dwordx4 %0, %1, off\n\ts_waitcnt vmcnt(0)" : "=&{v[236:239]}"(v) : "v"(mp) : "memory");
  } else {
    const unsigned off = ex_addr((unsigned)(s & 3) * 8192u + w * 1024 + l * 16, cx.pol);
    unsigned t_, cnt;
    asm volatile(
      "s_mov_b32 %[cnt], 0\n\t"
      "L%=_1:\n\t"
      "buffer_load_dwordx4 v[236:239], %[off], %[rs], %[r0] offen sc1\n\t"
      "s_waitcnt vmcnt(0)\n\t"
      "v_max3_u32 %[t], v236, v237, v238\n\t"
      "v_max_u32 %[t], %[t], v239\n\t"
      "v_cmp_eq_u32 vcc, -1, %[t]\n\t"
      "s_cbranch_vccz L%=_20\n\t"
      "s_add_u32 %[cnt], %[cnt], 1\n\t"
      "buffer_load_dwordx4 v[236:239], %[off], %[rs], %[r1] offen sc1\n\t"
      "s_waitcnt vmcnt(0)\n\t"
      "v_max3_u32 %[t], v236, v237, v238\n\t"
      "v_max_u32 %[t], %[t], v239\n\t"
      "v_cmp_eq_u32 vcc, -1, %[t]\n\t"
      "s_cbranch_vccz L%=_20\n\t"
      "s_add_u32 %[cnt], %[cnt], 1\n\t"
      "buffer_load_dwordx4 v[236:239], %[off], %[rs], %[r2] offen sc1\n\t"
      "s_waitcnt vmcnt(0)\n\t"
      "v_max3_u32 %[t], v236, v237, v238\n\t"
      "v_max_u32 %[t], %[t], v239\n\t"
      "v_cmp_eq_u32 vcc, -1, %[t]\n\t"
      "s_cbranch_vccz L%=_20\n\t"
      "s_add_u32 %[cnt], %[cnt], 1\n\t"
      "buffer_load_dwordx4 v[236:239], %[off], %[rs], %[r3] offen sc1\n\t"
      "s_waitcnt vmcnt(0)\n\t"
      "v_max3_u32 %[t], v236, v237, v238\n\t"
      "v_max_u32 %[t], %[t], v239\n\t"
      "v_cmp_eq_u32 vcc, -1, %[t]\n\t"
      "s_cbranch_vccz L%=_20\n\t"
      "s_add_u32 %[cnt], %[cnt], 1\n\t"
      "s_cmp_lt_u32 %[cnt], %[cap]\n\t"
      "s_cbranch_scc1 L%=_1\n\t"
      "L%=_20:\n\t"
      : "=&{v[236:239]}"(v), [t] "=&v"(t_), [cnt] "=&s"(cnt)
      : [off] "v"(off), [rs] "s"(cx.exs), [cap] "s"(SPIN_CAP), [r0] "s"(rep_off(0, cx.pol)), [r1] "s"(rep_off(1, cx.pol)), [r2] "s"(rep_off(2, cx.pol)), [r3] "s"(rep_off(3, cx.pol))
      : "vcc", "scc", "memory");
    if (cnt >= SPIN_CAP) __hip_atomic_store(cx.ctrl + C_TMO, 1u, RLX_AGENT);
    miss = cnt;
  }
  LAS unsigned char* mb = MEMIMG + l * 16;
  *(LAS u32x4*)(mb + w * 1024) = v;
  const int idx = (l & 8) ? 0xEEEE : 0x4444;
  f32x4 a0 = {0.f, 0.f, 0.f, 0.f}, a1 = {0.f, 0.f, 0.f, 0.f};
  a0 = smfma(v, W[0][0], a0, idx);
  a1 = smfma(v, W[1][0], a1, idx);
  asm volatile("" : "+v"(a0), "+v"(a1));
  BAR();
  u32x4 a[7];
#pragma unroll
  for (int i = 1; i < 8; ++i) a[i - 1] = *(LAS u32x4*)(mb + ((w + i) & 7) * 1024);
  __builtin_amdgcn_sched_barrier(0);
#pragma unroll
  for (int i = 1; i < 8; ++i) {
    a0 = smfma(a[i - 1], W[0][i], a0, idx);
    a1 = smfma(a[i - 1], W[1][i], a1, idx);
    if (i == 5) {
      asm volatile("" : "+v"(a0), "+v"(a1));
      tmid = now32();
      hook();
      __builtin_amdgcn_sched_barrier(0);
    }
  }
  float o[4];
#pragma unroll
  for (int r = 0; r < 4; ++r) {
    auto sw = __builtin_amdgcn_permlane32_swap(f2u(a0[r]), f2u(a1[r]), false, false);
    o[r] = u2f(sw[0]) + u2f(sw[1]);
  }
  u32x2 res; res.x = pk2(o[0], o[1]); res.y = pk2(o[2], o[3]);
  return res;
}

constexpr float SCALE = 0.08838834764831845f;
constexpr float LOG2E = 1.4426950408889634f;
__device__ __forceinline__ float vmax(float a, float b) { float r; asm("v_max_f32 %0, %1, %2" : "=v"(r) : "v"(a), "v"(b)); return r; }
__device__ __forceinline__ float vmax3(float a, float b, float c) { float r; asm("v_max3_f32 %0, %1, %2, %3" : "=v"(r) : "v"(a), "v"(b), "v"(c)); return r; }
template <bool MAXOP> __device__ __forceinline__ float red_g(float v) {
  {
    auto r = __builtin_amdgcn_permlane16_swap(f2u(v), f2u(v), false, false);
    const float a = u2f(r[0]), b = u2f(r[1]);
    v = MAXOP ? vmax(a, b) : a + b;
  }
  {
    auto r = __builtin_amdgcn_permlane32_swap(f2u(v), f2u(v), false, false);
    const float a = u2f(r[0]), b = u2f(r[1]);
    v = MAXOP ? vmax(a, b) : a + b;
  }
  return v;
}

template <int ROLE>
__device__ __forceinline__ void head_loop(const Params& p, const Ctx& cx_, LAS unsigned char* L, int h, const u32x8 (&W)[2][8]) {
  const Ctx cx = fresh(cx_);
  const int w = cx.wave, l = cx.lane, g = l >> 4, n = l & 15;
  LAS unsigned char* MEMIMG = L;
  LAS unsigned char* QKIMG = L + 8192;
  LAS unsigned* XCNT = (LAS unsigned*)(L + 12288);
  LAS unsigned char* KQ = L + 16384;
  auto stage = [&](int s) {
    wait_words(cx, C_KQ + (s >> 1) * 32, 1, 64u);
    const unsigned kb = (unsigned)((w == 4 ? OFF_KX : OFF_QX) + ((size_t)s * NH + h) * 8192) + l * 16;
    LAS unsigned char* d = KQ + (s & 1) * 16384 + (w == 4 ? 0 : 8192) + l * 16;
    u32x4 pf[8];
#pragma unroll
    for (int i = 0; i < 8; ++i) pf[i] = ld_sc1(cx.ws, kb + i * 1024);
#pragma unroll
    for (int i = 0; i < 8; ++i) *(LAS u32x4*)(d + i * 1024) = pf[i];
  };
  if (ROLE == 2) stage(0);
  unsigned tref = 0; int Dh = p.dh;
  const int q4 = (l & 15) >> 2, pp = l & 3;
  LAS unsigned char* trb = QKIMG + (16 * (g >> 1) + 4 * (g & 1) + q4) * 32;
  const bool up = n >= 8;
  LAS unsigned char* qkw = QKIMG + (((2 * w + (l >> 5)) & 7) * 16 + n) * 32 + (w >> 2) * 16 + (g & 1) * 8;
  const unsigned pboff = PBASE + h * 640 + (n & 7) * 16 + (up ? 512 : g * 128);

  for (int s = 0; s < NSEG; ++s) {
    if (s > 0) timed_wait(tref, Dh);
    __builtin_amdgcn_s_setprio(2);
    unsigned miss = 0;
    const u32x2 qk = front(p, cx, MEMIMG, W, s, miss, tref, [] {});
    Dh = adapt(Dh, p.dh, miss, s);
    *(LAS u32x2*)qkw = qk;
    LAS unsigned char* KI = KQ + (s & 1) * 16384 + g * 512 + n * 16;
    if (ROLE == 0) {
      u32x4 Kf[2][4];
      {
#pragma unroll
      for (int t = 0; t < 4; ++t) { Kf[0][t] = *(LAS u32x4*)(KI + t * 2048); Kf[1][t] = *(LAS u32x4*)(KI + t * 2048 + 256); }
      }
      unsigned paddr = ex_addr((unsigned)(s & 3) * PSLOT + pboff, cx.pol);
      asm volatile("" : "+v"(paddr));
      BAR();
      __builtin_amdgcn_s_setprio(3);
      if (s == NSEG - 1) break;
      u32x4 F[4];
#pragma unroll
      for (int t = 0; t < 4; ++t) {
        const u32x2 lo = tr_read(trb + t * 1024 + pp * 8), hi = tr_read(trb + t * 1024 + 256 + pp * 8);
        F[t] = (u32x4){lo.x, lo.y, hi.x, hi.y};
      }
      __builtin_amdgcn_sched_barrier(0);
      f32x4 s0 = {0.f, 0.f, 0.f, 0.f}, s1 = s0, sm = s0;
#pragma unroll
      for (int t = 0; t < 4; ++t) { s1 = mfma16(Kf[1][t], F[t], s1); sm = mfma16(F[t], F[t], sm); s0 = mfma16(Kf[0][t], F[t], s0); }
      float v[6];
#pragma unroll
      for (int r = 0; r < 4; ++r) v[r] = u2f((unsigned)__builtin_amdgcn_update_dpp((int)f2u(s0[r]), (int)f2u(s1[r]), 0x128, 0xf, 0xc, false));
      v[4] = u2f((unsigned)__builtin_amdgcn_update_dpp((int)f2u(sm[0]), (int)f2u(sm[2]), 0x128, 0xf, 0xc, false));
      v[5] = u2f((unsigned)__builtin_amdgcn_update_dpp((int)f2u(sm[1]), (int)f2u(sm[3]), 0x128, 0xf, 0xc, false));
      v[4] = (g >= 2) ? v[4] : -INFINITY;
      v[5] = (g >= 2) ? v[5] : -INFINITY;
      float mx = vmax3(vmax3(v[0], v[1], v[2]), v[3], vmax(v[4], v[5]));
      mx = vmax(mx, u2f(dpp_ror8(f2u(mx))));
      mx = red_g<true>(mx);
      const float cs = SCALE * LOG2E, mc = -mx * cs;
#pragma unroll
      for (int i = 0; i < 6; ++i) v[i] = __builtin_amdgcn_exp2f(__builtin_fmaf(v[i], cs, mc));
      float sum = ((v[0] + v[1]) + (v[2] + v[3])) + (v[4] + v[5]);
      sum += u2f(dpp_ror8(f2u(sum)));
      sum = red_g<false>(sum);
      const float inv = __builtin_amdgcn_rcpf(sum);
      const unsigned px0 = nosent(pk2(v[0] * inv, v[1] * inv)), px1 = nosent(pk2(v[2] * inv, v[3] * inv));
      const unsigned pm = nosent(pk2(v[4] * inv, v[5] * inv));
      const unsigned o0 = dpp_ror8(px0), o1 = dpp_ror8(px1), t1 = dpp_ror8(pm);
      const unsigned d2 = __builtin_amdgcn_permlane16_swap(t1, t1, false, false)[1], d3 = __builtin_amdgcn_permlane16_swap(pm, pm, false, false)[1];
      u32x4 dat; dat.x = up ? t1 : px0; dat.y = up ? pm : px1; dat.z = up ? d2 : o0; dat.w = up ? d3 : o1;
      if (!up || g == 2) {
#pragma unroll
        for (int r = 0; r < RP; ++r) st_sc1s(cx.ex, paddr, rep_off(r, cx.pol), dat);
      }
      __builtin_amdgcn_s_setprio(0);
      if (l < 40) {
        const unsigned ra = ex_addr(PBASE + (unsigned)((s + 2) & 3) * PSLOT + h * 640 + l * 16, cx.pol);
#pragma unroll
        for (int r = 0; r < RP; ++r) st_sc1s(cx.ex, ra, rep_off(r, cx.pol), (u32x4){SENT, SENT, SENT, SENT});
      }
    } else {
      BAR();
      __builtin_amdgcn_s_setprio(0);
      if (ROLE == 2) {
        __builtin_amdgcn_s_sleep(3);
        if (s + 1 < NSEG) stage(s + 1);
      } else if (ROLE == 3) {
        __builtin_amdgcn_s_sleep(3);
        const int qt = w - 5;
        u32x4 F[4], Kf[2][4], Qf[4];
#pragma unroll
        for (int t = 0; t < 4; ++t) {
          const u32x2 lo = tr_read(trb + t * 1024 + pp * 8), hi = tr_read(trb + t * 1024 + 256 + pp * 8);
          F[t] = (u32x4){lo.x, lo.y, hi.x, hi.y};
          Kf[0][t] = *(LAS u32x4*)(KI + t * 2048); Kf[1][t] = *(LAS u32x4*)(KI + t * 2048 + 256);
          Qf[t] = *(LAS u32x4*)(KI + 8192 + t * 2048 + qt * 256);
        }
        f32x4 s0 = {0.f, 0.f, 0.f, 0.f}, s1 = s0, sm = s0;
#pragma unroll
        for (int t = 0; t < 4; ++t) { s0 = mfma16(Kf[0][t], Qf[t], s0); s1 = mfma16(Kf[1][t], Qf[t], s1); sm = mfma16(F[t], Qf[t], sm); }
        const int qi = qt * 16 + n;
        float sc[12];
#pragma unroll
        for (int r = 0; r < 4; ++r) {
          sc[r] = (4 * g + r <= qi) ? s0[r] : -INFINITY;
          sc[4 + r] = (16 + 4 * g + r <= qi) ? s1[r] : -INFINITY;
          sc[8 + r] = (g >= 2) ? sm[r] : -INFINITY;
        }
        float mx = vmax3(vmax3(sc[0], sc[1], sc[2]), vmax3(sc[3], sc[4], sc[5]), vmax3(vmax3(sc[6], sc[7], sc[8]), vmax3(sc[9], sc[10], sc[11]), sc[0]));
        mx = red_g<true>(mx);
        const float cs = SCALE * LOG2E, mc = -mx * cs;
        float sum = 0.f;
#pragma unroll
        for (int i = 0; i < 12; ++i) { sc[i] = __builtin_amdgcn_exp2f(__builtin_fmaf(sc[i], cs, mc)); sum += sc[i]; }
        sum = red_g<false>(sum);
        const float inv = __builtin_amdgcn_rcpf(sum);
        u32x4 px; px.x = pk2(sc[0] * inv, sc[1] * inv); px.y = pk2(sc[2] * inv, sc[3] * inv); px.z = pk2(sc[4] * inv, sc[5] * inv); px.w = pk2(sc[6] * inv, sc[7] * inv);
        u32x2 pm; pm.x = pk2(sc[8] * inv, sc[9] * inv); pm.y = pk2(sc[10] * inv, sc[11] * inv);
        const unsigned pb = (unsigned)(OFF_PX + ((size_t)s * NH + h) * 2560) + qi * 16;
        st_sc1(cx.ws, pb + g * 512, px);
        if (g >= 2) st_sc1_64(cx.ws, pb + 4 * 512 + (g - 2) * 8, pm);
        DRAIN();
        if (l == 0) {
          const unsigned old = __hip_atomic_fetch_add(XCNT, 1u, __ATOMIC_RELAXED, __HIP_MEMORY_SCOPE_WORKGROUP);
          if (old & 1u) st_sc1_32(cx.ex, ex_addr(FPU + h * 128, cx.pol) + rep_off(0, cx.pol), (unsigned)s);
        }
      }
    }
  }
}

__device__ __forceinline__ void chain_ready(const Ctx& cx_, int id) {
  const Ctx cx = fresh(cx_);
  if (cx.wave == 0) wait_words(cx, C_RDY, 8, 12u);
  BAR();
}

__device__ __forceinline__ void head_role(const Params& p, const Ctx& cx_, LAS unsigned char* L, int h) {
  Ctx cx = fresh(cx_);
  const int w = cx.wave, l = cx.lane, g = l >> 4, tid = w * 64 + l;
  u32x8 W[2][8];
#pragma unroll
  for (int rd = 0; rd < 2; ++rd) {
    const u16* Wsrc = rd ? p.Wk : p.Wq;
#pragma unroll
    for (int i = 0; i < 16; ++i) {
      const int kap = i * 4 + (l >> 4), sig = l & 15;
      const int gsw = (kap & 3) | (((kap >> 3) & 1) << 2);
      const int chunk = (((sig >> 1) ^ gsw) << 1) | (sig & 1);
      const int d = 64 * w + 16 * (kap >> 4) + 8 * ((kap >> 1) & 1) + 2 * ((kap >> 2) & 3) + (kap & 1);
      GLDS(Wsrc + (size_t)d * HD + h * DH + chunk * 8, L + w * 16384 + i * 1024);
    }
    if (rd == 0) {
      cx.pol = get_pol(cx);
      if (tid == 511) st_sc1_32(cx.ex, ex_addr(FPU + h * 128, cx.pol) + rep_off(0, cx.pol), SENT);
#pragma unroll
      for (int k = 0; k < 2; ++k) {
        const int q = tid + k * 512;
        if (q < 4 * RP * 40) {
          const int pc = q % 40, sr = q / 40;
          st_sc1(cx.ex, ex_addr(PBASE + (unsigned)(sr & 3) * PSLOT + h * 640 + pc * 16, cx.pol) + rep_off((unsigned)(sr >> 2), cx.pol), (u32x4){SENT, SENT, SENT, SENT});
        }
      }
    }
    DRAIN();
    BAR();
    if (rd == 0 && tid == 0) arrive(cx, C_RDY + (h & 7) * 32);
    if ((w >> 2) == rd) {
      const int q = (l & 15) >> 2, pp = l & 3;
#pragma unroll
      for (int i = 0; i < 8; ++i) {
        LAS unsigned char* img = L + ((w + i) & 7) * 16384;
#pragma unroll
        for (int t = 0; t < 2; ++t) {
          const int et = (2 * w + t) & 7;
          u32x8 f;
#pragma unroll
          for (int jq = 0; jq < 4; ++jq) {
            const int kap = (jq >> 1) * 32 + 8 * g + (jq & 1) * 4 + q;
            const int gsw = (kap & 3) | (((kap >> 3) & 1) << 2);
            const u32x2 v = tr_read(img + kap * 256 + ((et ^ gsw) * 32) + pp * 8);
            f[jq * 2] = v.x; f[jq * 2 + 1] = v.y;
          }
          W[t][i] = f;
        }
      }
    }
    BAR();
  }
  if (tid < 4) *(LAS unsigned*)(L + 12288 + tid * 4) = 0u;

  chain_ready(cx, h);

  if (w == 0) head_loop<0>(p, cx, L, h, W);
  else if (w < 4) head_loop<1>(p, cx, L, h, W);
  else if (w == 4 || w == 7) head_loop<2>(p, cx, L, h, W);
  else head_loop<3>(p, cx, L, h, W);
}

__device__ __forceinline__ void wo_issue(const Params& p, const Ctx& cx_, LAS unsigned char* L, int pid) {
  const Ctx cx = fresh(cx_);
  const int w = cx.wave, l = cx.lane, h = pid & 31, qr = pid >> 5;
#pragma unroll
  for (int i = 0; i < 4; ++i) {
    const int r = (w * 4 + i) * 4 + (l >> 4);
    GLDS(p.Wv + (size_t)(128 * qr + r) * HD + h * DH + ((l & 15) ^ (r & 15)) * 8, L + 131072 + (w * 4 + i) * 1024);
  }
#pragma unroll
  for (int i = 0; i < 16; ++i) {
    const int e = w * 16 + i;
    GLDS(p.Wo + ((size_t)h * DH + e) * DM + ((((l >> 2) ^ (i & 3)) << 2) + (l & 3)) * 8, L + e * 1024);
  }
}
__device__ __forceinline__ void wvo_part(const Params& p, const Ctx& cx_, LAS unsigned char* L, int pid, u32x4 (&B)[2][8]) {
  const Ctx cx = fresh(cx_);
  const int w = cx.wave, l = cx.lane, hi = l >> 5, h = pid & 31, qr = pid >> 5;
  {
    const int blk = (l >> 4) & 1, q = (l & 15) >> 2, pp = l & 3;
#pragma unroll
    for (int nt = 0; nt < 2; ++nt) {
      LAS unsigned char* b0 = L + (4 * hi + q) * 1024 + ((2 * w + nt) ^ q) * 64 + blk * 32 + pp * 8;
#pragma unroll
      for (int u = 0; u < 8; ++u) {
        const u32x2 lo = tr_read(b0 + u * 16384), hi2 = tr_read(b0 + u * 16384 + 8192);
        B[nt][u] = (u32x4){lo.x, lo.y, hi2.x, hi2.y};
      }
    }
  }
#pragma unroll
  for (int i = 0; i < 4; ++i) {
    const int dt = qr * 4 + i;
    u32x4 A[8];
    LAS unsigned char* a0 = L + 131072 + (32 * i + (l & 31)) * 256 + hi * 8;
#pragma unroll
    for (int u = 0; u < 8; ++u) {
      const u32x2 lo = *(LAS u32x2*)(a0 + (((2 * u) ^ (l & 15)) << 4)), h2 = *(LAS u32x2*)(a0 + (((2 * u + 1) ^ (l & 15)) << 4));
      A[u] = (u32x4){lo.x, lo.y, h2.x, h2.y};
    }
#pragma unroll
    for (int nt = 0; nt < 2; ++nt) {
      f32x16 acc = {0.f};
#pragma unroll
      for (int u = 0; u < 8; ++u) acc = mfma32(A[u], B[nt][u], acc);
      const int col = 64 * w + 32 * nt + (l & 31);
#pragma unroll
      for (int b16 = 0; b16 < 2; ++b16) {
        const int d16 = 2 * dt + b16, T = d16 >> 2, gA = d16 & 3;
        const int lane2 = (2 * (gA & 1) + hi) * 16 + (h & 1) * 8 + (col & 7);
        u32x4 o;
        o.x = pk2(acc[8 * b16 + 0], acc[8 * b16 + 1]); o.y = pk2(acc[8 * b16 + 4], acc[8 * b16 + 5]);
        o.z = pk2(acc[8 * b16 + 2], acc[8 * b16 + 3]); o.w = pk2(acc[8 * b16 + 6], acc[8 * b16 + 7]);
        st_sc1(cx.ws, (unsigned)(OFF_WVO + ((((size_t)(col >> 3) * 16 + (h >> 1)) * 8 + T) * 2048) + (gA >> 1) * 1024 + lane2 * 16), o);
      }
    }
  }

  DRAIN();
  BAR();
  if (w == 0 && l == 0) arrive(cx, C_WVO + (pid & 7) * 32);
}

template <int ROLE>
__device__ __forceinline__ void col_loop(const Params& p, const Ctx& cx_, LAS unsigned char* L, int c, const u32x8 (&W)[2][8]) {
  const Ctx cx = fresh(cx_);
  const int w = cx.wave, l = cx.lane, g = l >> 4, n = l & 15, tid = w * 64 + l;
  LAS unsigned char* MEMIMG = L;
  LAS unsigned* UCNT = (LAS unsigned*)(L + 8192);
  LAS unsigned char* RED = L + 9216;
  LAS unsigned char* UIMG = L + 12288;
  const int ht = tid - 256;
  unsigned uoff[4];
#pragma unroll
  for (int i = 0; i < 4; ++i) { const int ch4 = (ht >> 3) + 32 * i; uoff[i] = (unsigned)((((ch4 >> 2) * 5 + (ch4 & 3)) * 8 + (ht & 7)) * 16); }
  u32x4 ur[4];
  auto uload = [&](int s) {
    const unsigned base = (unsigned)(OFF_U + ((size_t)s * NCOL + c) * U_SC);
#pragma unroll
    for (int i = 0; i < 4; ++i) ur[i] = ld_sc1(cx.ws, base + uoff[i]);
  };
  auto ustore = [&](int s) {
    LAS unsigned char* UI = UIMG + (s & 1) * 20480;
#pragma unroll
    for (int i = 0; i < 4; ++i) *(LAS u32x4*)(UI + uoff[i]) = ur[i];
  };
  if (ROLE == 2) { wait_words(cx, C_U, 1, 32u); uload(0); ustore(0); }
  unsigned tb1 = 0, tpub = 0; int Dp = p.dp; bool unext = false;
  const unsigned umo2 = (unsigned)((((ht >> 3) * 5 + 4) * 8 + (ht & 7)) * 16);
  const unsigned umo = (unsigned)((((2 * (2 * w + (l >> 5)) + (n >> 3)) * 5 + 4) * 8 + (n & 7)) * 16 + (g & 1) * 8);
  for (int s = 0; s < NSEG; ++s) {
    LAS unsigned char* UI = UIMG + (s & 1) * 20480;
    unsigned miss = 0;
    u32x4 umprev = {0, 0, 0, 0};
    if (ROLE == 2 && s > 0) umprev = *(LAS u32x4*)(UIMG + ((s - 1) & 1) * 20480 + umo2);
    if (s > 0) timed_wait(tpub, p.dm);
    const u32x2 um = front(p, cx, MEMIMG, W, s, miss, tb1, [&] {
      if (ROLE == 2) {
        if (unext && s + 1 < NSEG - 1) uload(s + 1);
        if (s > 0) st_sc1(cx.ws, (unsigned)(OFF_U + ((size_t)(s - 1) * NCOL + c) * U_SC) + umo2, umprev);
      }
    });
    *(LAS u32x2*)(UI + umo) = um;
    BAR();
    if (ROLE == 2) {
      if (s == NSEG - 1) st_sc1(cx.ws, (unsigned)(OFF_U + ((size_t)s * NCOL + c) * U_SC) + umo2, *(LAS u32x4*)(UI + umo2));
      if (!unext && s + 1 < NSEG - 1) { wait_words(cx, C_U + (s + 1) * 32, 1, 32u); uload(s + 1); }
      DRAIN();
      if (s == NSEG - 1) {
        if (l == 0) {
          const unsigned old = __hip_atomic_fetch_add(UCNT, 1u, __ATOMIC_RELAXED, __HIP_MEMORY_SCOPE_WORKGROUP);
          if (old == 3u) st_sc1_32(cx.ex, ex_addr(FUMU + c * 128, cx.pol) + rep_off(0, cx.pol), (unsigned)s);
        }
        break;
      }
      if (s + 1 < NSEG - 1) ustore(s + 1);
      unext = (s + 2 < NSEG - 1) && test_word(cx, C_U + (s + 2) * 32, 32u);
    } else {
      if (s == NSEG - 1) break;
      u32x4 B[5];
      unsigned aoff[5];
#pragma unroll
      for (int i = 0; i < 5; ++i) {
        const int ch = (n >> 3) * 80 + 4 * (w * 5 + i) + g;
        B[i] = *(LAS u32x4*)(UI + (ch * 8 + (n & 7)) * 16);
        aoff[i] = ex_addr(PBASE + (unsigned)(s & 3) * PSLOT + ch * 128 + (n & 7) * 16, cx.pol);
      }
      timed_wait(tb1, Dp);
      __builtin_amdgcn_s_setprio(3);
      u32x4 A0[5]; f32x4 acc; unsigned t_, cnt;
      asm volatile(
        "s_mov_b32 %[cnt], 0\n\t"
        "L%=_1:\n\t"
        "buffer_load_dwordx4 v[236:239], %[o0], %[rs], %[r0] offen sc1\n\t"
        "buffer_load_dwordx4 v[240:243], %[o1], %[rs], %[r0] offen sc1\n\t"
        "buffer_load_dwordx4 v[244:247], %[o2], %[rs], %[r0] offen sc1\n\t"
        "buffer_load_dwordx4 v[248:251], %[o3], %[rs], %[r0] offen sc1\n\t"
        "buffer_load_dwordx4 v[252:255], %[o4], %[rs], %[r0] offen sc1\n\t"
        "s_waitcnt vmcnt(4)\n\t"
        "v_mfma_f32_16x16x32_bf16 %[acc], v[236:239], %[b0], 0\n\t"
        "v_max3_u32 %[t], v236, v237, v238\n\t"
        "v_max_u32 %[t], %[t], v239\n\t"
        "s_waitcnt vmcnt(3)\n\t"
        "v_mfma_f32_16x16x32_bf16 %[acc], v[240:243], %[b1], %[acc]\n\t"
        "v_max3_u32 %[t], %[t], v240, v241\n\t"
        "v_max3_u32 %[t], %[t], v242, v243\n\t"
        "s_waitcnt vmcnt(2)\n\t"
        "v_mfma_f32_16x16x32_bf16 %[acc], v[244:247], %[b2], %[acc]\n\t"
        "v_max3_u32 %[t], %[t], v244, v245\n\t"
        "v_max3_u32 %[t], %[t], v246, v247\n\t"
        "s_waitcnt vmcnt(1)\n\t"
        "v_mfma_f32_16x16x32_bf16 %[acc], v[248:251], %[b3], %[acc]\n\t"
        "v_max3_u32 %[t], %[t], v248, v249\n\t"
        "v_max3_u32 %[t], %[t], v250, v251\n\t"
        "s_waitcnt vmcnt(0)\n\t"
        "v_mfma_f32_16x16x32_bf16 %[acc], v[252:255], %[b4], %[acc]\n\t"
        "v_max3_u32 %[t], %[t], v252, v253\n\t"
        "v_max3_u32 %[t], %[t], v254, v255\n\t"
        "v_cmp_eq_u32 vcc, -1, %[t]\n\t"
        "s_cbranch_vccz L%=_20\n\t"
        "s_add_u32 %[cnt], %[cnt], 1\n\t"
        "buffer_load_dwordx4 v[236:239], %[o0], %[rs], %[r1] offen sc1\n\t"
        "buffer_load_dwordx4 v[240:243], %[o1], %[rs], %[r1] offen sc1\n\t"
        "buffer_load_dwordx4 v[244:247], %[o2], %[rs], %[r1] offen sc1\n\t"
        "buffer_load_dwordx4 v[248:251], %[o3], %[rs], %[r1] offen sc1\n\t"
        "buffer_load_dwordx4 v[252:255], %[o4], %[rs], %[r1] offen sc1\n\t"
        "s_waitcnt vmcnt(4)\n\t"
        "v_mfma_f32_16x16x32_bf16 %[acc], v[236:239], %[b0], 0\n\t"
        "v_max3_u32 %[t], v236, v237, v238\n\t"
        "v_max_u32 %[t], %[t], v239\n\t"
        "s_waitcnt vmcnt(3)\n\t"
        "v_mfma_f32_16x16x32_bf16 %[acc], v[240:243], %[b1], %[acc]\n\t"
        "v_max3_u32 %[t], %[t], v240, v241\n\t"
        "v_max3_u32 %[t], %[t], v242, v243\n\t"
        "s_waitcnt vmcnt(2)\n\t"
        "v_mfma_f32_16x16x32_bf16 %[acc], v[244:247], %[b2], %[acc]\n\t"
        "v_max3_u32 %[t], %[t], v244, v245\n\t"
        "v_max3_u32 %[t], %[t], v246, v247\n\t"
        "s_waitcnt vmcnt(1)\n\t"
        "v_mfma_f32_16x16x32_bf16 %[acc], v[248:251], %[b3], %[acc]\n\t"
        "v_max3_u32 %[t], %[t], v248, v249\n\t"
        "v_max3_u32 %[t], %[t], v250, v251\n\t"
        "s_waitcnt vmcnt(0)\n\t"
        "v_mfma_f32_16x16x32_bf16 %[acc], v[252:255], %[b4], %[acc]\n\t"
        "v_max3_u32 %[t], %[t], v252, v253\n\t"
        "v_max3_u32 %[t], %[t], v254, v255\n\t"
        "v_cmp_eq_u32 vcc, -1, %[t]\n\t"
        "s_cbranch_vccz L%=_20\n\t"
        "s_add_u32 %[cnt], %[cnt], 1\n\t"
        "buffer_load_dwordx4 v[236:239], %[o0], %[rs], %[r2] offen sc1\n\t"
        "buffer_load_dwordx4 v[240:243], %[o1], %[rs], %[r2] offen sc1\n\t"
        "buffer_load_dwordx4 v[244:247], %[o2], %[rs], %[r2] offen sc1\n\t"
        "buffer_load_dwordx4 v[248:251], %[o3], %[rs], %[r2] offen sc1\n\t"
        "buffer_load_dwordx4 v[252:255], %[o4], %[rs], %[r2] offen sc1\n\t"
        "s_waitcnt vmcnt(4)\n\t"
        "v_mfma_f32_16x16x32_bf16 %[acc], v[236:239], %[b0], 0\n\t"
        "v_max3_u32 %[t], v236, v237, v238\n\t"
        "v_max_u32 %[t], %[t], v239\n\t"
        "s_waitcnt vmcnt(3)\n\t"
        "v_mfma_f32_16x16x32_bf16 %[acc], v[240:243], %[b1], %[acc]\n\t"
        "v_max3_u32 %[t], %[t], v240, v241\n\t"
        "v_max3_u32 %[t], %[t], v242, v243\n\t"
        "s_waitcnt vmcnt(2)\n\t"
        "v_mfma_f32_16x16x32_bf16 %[acc], v[244:247], %[b2], %[acc]\n\t"
        "v_max3_u32 %[t], %[t], v244, v245\n\t"
        "v_max3_u32 %[t], %[t], v246, v247\n\t"
        "s_waitcnt vmcnt(1)\n\t"
        "v_mfma_f32_16x16x32_bf16 %[acc], v[248:251], %[b3], %[acc]\n\t"
        "v_max3_u32 %[t], %[t], v248, v249\n\t"
        "v_max3_u32 %[t], %[t], v250, v251\n\t"
        "s_waitcnt vmcnt(0)\n\t"
        "v_mfma_f32_16x16x32_bf16 %[acc], v[252:255], %[b4], %[acc]\n\t"
        "v_max3_u32 %[t], %[t], v252, v253\n\t"
        "v_max3_u32 %[t], %[t], v254, v255\n\t"
        "v_cmp_eq_u32 vcc, -1, %[t]\n\t"
        "s_cbranch_vccz L%=_20\n\t"
        "s_add_u32 %[cnt], %[cnt], 1\n\t"
        "s_cmp_lt_u32 %[cnt], %[cap]\n\t"
        "s_cbranch_scc1 L%=_1\n\t"
        "L%=_20:\n\t"
        "s_nop 7\n\t"
        : "=&{v[236:239]}"(A0[0]), "=&{v[240:243]}"(A0[1]), "=&{v[244:247]}"(A0[2]), "=&{v[248:251]}"(A0[3]), "=&{v[252:255]}"(A0[4]), [acc] "=&v"(acc), [t] "=&v"(t_), [cnt] "=&s"(cnt)
        : [o0] "v"(aoff[0]), [o1] "v"(aoff[1]), [o2] "v"(aoff[2]), [o3] "v"(aoff[3]), [o4] "v"(aoff[4]), [b0] "v"(B[0]), [b1] "v"(B[1]), [b2] "v"(B[2]), [b3] "v"(B[3]), [b4] "v"(B[4]),
          [rs] "s"(cx.exs), [cap] "s"(SPIN_CAP), [r0] "s"(rep_off(0, cx.pol)), [r1] "s"(rep_off(1, cx.pol)), [r2] "s"(rep_off(2, cx.pol))
        : "vcc", "scc", "memory");
      if (cnt >= SPIN_CAP) __hip_atomic_store(cx.ctrl + C_TMO, 1u, RLX_AGENT);
      if ((n >> 3) == (g >> 1)) *(LAS f32x4*)(RED + (((w * 2 + (g >> 1)) * 8 + (n & 7)) * 8 + 4 * (g & 1)) * 4) = acc;
      Dp = adapt(Dp, p.dp, cnt, s);
    }
    BAR();
    tpub = (unsigned)__builtin_amdgcn_s_memtime();
    if (ROLE == 2 && s > 0 && tid == 256) st_sc1_32(cx.ex, ex_addr(FUMU + c * 128, cx.pol) + rep_off(0, cx.pol), (unsigned)(s - 1));
    if (ROLE == 0) {
      const int m = l >> 3, c8 = l & 7;
      float x[8];
#pragma unroll
      for (int sl = 0; sl < 8; ++sl) x[sl] = *(LAS float*)(RED + ((sl * 8 + c8) * 8 + m) * 4);
      const float v = ((x[0] + x[1]) + (x[2] + x[3])) + ((x[4] + x[5]) + (x[6] + x[7]));
      const float o = u2f(__builtin_amdgcn_update_dpp(0, f2u(v), 0xB1, 0xf, 0xf, true));
      const unsigned d = nosent(pk2(v, o));
      u32x4 dat; dat.x = d;
      dat.y = (unsigned)__builtin_amdgcn_update_dpp(0, (int)d, 0xAA, 0xf, 0xf, true);
      dat.z = (unsigned)__builtin_amdgcn_update_dpp(0, (int)d, 0x104, 0xf, 0xf, true);
      dat.w = (unsigned)__builtin_amdgcn_update_dpp(0, (int)dat.y, 0x104, 0xf, 0xf, true);
      if (c8 == 0) {
        const unsigned ma = ex_addr((unsigned)((s + 1) & 3) * 8192u + c * 128 + m * 16, cx.pol);
#pragma unroll
        for (int r = 0; r < RM; ++r) st_sc1s(cx.ex, ma, rep_off(r, cx.pol), dat);
      }
      if (l < 8 * RM) st_sc1(cx.ex, ex_addr((unsigned)((s + 3) & 3) * 8192u + c * 128 + (l & 7) * 16, cx.pol) + rep_off((unsigned)(l >> 3), cx.pol), (u32x4){SENT, SENT, SENT, SENT});
    }
    __builtin_amdgcn_s_setprio(0);
  }
}

__device__ __forceinline__ void col_role(const Params& p, const Ctx& cx0, LAS unsigned char* L, int c) {
  const int w = cx0.wave;
  Ctx cx_ = cx0;
  {
    u32x4 B[2][8];
    wo_issue(p, cx_, L, 64 + c);
    DRAIN();
    BAR();
    wvo_part(p, cx_, L, 64 + c, B);
  }
  cx_.pol = get_pol(fresh(cx_));
  if (w < 2) {
    const Ctx cx = fresh(cx_);
    const int tid = w * 64 + cx.lane;
    const int pc = tid & 7, sr = tid >> 3;
    if (tid == 0) st_sc1_32(cx.ex, ex_addr(FUMU + c * 128, cx.pol) + rep_off(0, cx.pol), SENT);
    st_sc1(cx.ex, ex_addr((unsigned)(sr & 3) * 8192u + c * 128 + pc * 16, cx.pol) + rep_off((unsigned)(sr >> 2), cx.pol), (u32x4){SENT, SENT, SENT, SENT});
    DRAIN();
  }
  BAR();
  const Ctx cx = fresh(cx_);
  const int l = cx.lane, tid = w * 64 + l;

  if (tid == 0) arrive(cx, C_RDY + (c & 7) * 32);
  if (w == 0) wait_words(cx, C_WVO, 8, 16u);
  BAR();

  u32x8 W[2][8];
#pragma unroll
  for (int t = 0; t < 2; ++t)
#pragma unroll
    for (int i = 0; i < 8; ++i) {
      const unsigned off = (unsigned)(OFF_WVO + (((size_t)c * 16 + 2 * w + t) * 8 + ((w + i) & 7)) * 2048) + l * 16;
      const u32x4 a = ld_sc1(cx.ws, off), b = ld_sc1(cx.ws, off + 1024);
      W[t][i] = (u32x8){a.x, a.y, a.z, a.w, b.x, b.y, b.z, b.w};
    }
  if (tid == 0) *(LAS unsigned*)(L + 8192) = 0u;
  chain_ready(cx, c);

  if (w == 0) col_loop<0>(p, cx, L, c, W);
  else if (w < 4) col_loop<1>(p, cx, L, c, W);
  else col_loop<2>(p, cx, L, c, W);
}

template <int CTRL, int RMASK> __device__ __forceinline__ unsigned dppu(unsigned old, unsigned x) { return (unsigned)__builtin_amdgcn_update_dpp((int)old, (int)x, CTRL, RMASK, 0xf, false); }
__device__ __forceinline__ float wave_sum(float v) {
  v += u2f(dppu<0x111, 0xf>(0u, f2u(v)));
  v += u2f(dppu<0x112, 0xf>(0u, f2u(v)));
  v += u2f(dppu<0x114, 0xf>(0u, f2u(v)));
  v += u2f(dppu<0x118, 0xf>(0u, f2u(v)));
  v += u2f(dppu<0x142, 0xa>(0u, f2u(v)));
  v += u2f(dppu<0x143, 0xc>(0u, f2u(v)));
  return u2f((unsigned)__builtin_amdgcn_readlane((int)f2u(v), 63));
}
__device__ __forceinline__ unsigned wave_umax(unsigned v) {
  v = max(v, dppu<0x111, 0xf>(0u, v));
  v = max(v, dppu<0x112, 0xf>(0u, v));
  v = max(v, dppu<0x114, 0xf>(0u, v));
  v = max(v, dppu<0x118, 0xf>(0u, v));
  v = max(v, dppu<0x142, 0xa>(0u, v));
  v = max(v, dppu<0x143, 0xc>(0u, v));
  return (unsigned)__builtin_amdgcn_readlane((int)v, 63);
}
template <int NR>
__device__ __forceinline__ void ln_part(const Params& p, const Ctx& cx_, int row0, int id) {
  const Ctx cx = fresh(cx_);
  const int w = cx.wave, l = cx.lane;
  const u32x4 wr = *(const GAS u32x4*)(p.w_r + l * 8);
  u32x4 xv[NR];
#pragma unroll
  for (int k = 0; k < NR; ++k) xv[k] = *(const GAS u32x4*)(p.x + (size_t)(row0 + NR * w + k) * DM + l * 8);
#pragma unroll
  for (int k = 0; k < NR; ++k) {
    const int row = row0 + NR * w + k;
    float v[8];
#pragma unroll
    for (int i = 0; i < 4; ++i) { v[2 * i] = bflo(xv[k][i]); v[2 * i + 1] = bfhi(xv[k][i]); }
    const float mean = wave_sum(((v[0] + v[1]) + (v[2] + v[3])) + ((v[4] + v[5]) + (v[6] + v[7]))) * (1.f / DM);
    float q = 0.f;
#pragma unroll
    for (int i = 0; i < 8; ++i) { const float d = v[i] - mean; q += d * d; }
    const float mb = bfr(mean), vb = bfr(wave_sum(q) * (1.f / DM));
    const float den = bfr(sqrtf(bfr(vb + 1e-5f)));
    float r = 0.f;
#pragma unroll
    for (int i = 0; i < 8; ++i) v[i] = bfr(bfr(v[i] - mb) / den);
    u32x4 o; o.x = pk2(v[0], v[1]); o.y = pk2(v[2], v[3]); o.z = pk2(v[4], v[5]); o.w = pk2(v[6], v[7]);
    st_sc1(cx.ws, (unsigned)(OFF_XN + (size_t)row * 1024) + l * 16, o);
#pragma unroll
    for (int i = 0; i < 4; ++i) r += v[2 * i] * bflo(wr[i]) + v[2 * i + 1] * bfhi(wr[i]);
    r = bfr(wave_sum(r));
    if (l == 0) st_sc1(cx.ws, (unsigned)(OFF_STAT + (size_t)row * 16), (u32x4){f2u(mb), f2u(den), f2u(r), 0u});
  }
  DRAIN();
  BAR();
  if (w == 0 && l == 0) arrive(cx, C_XN + (id & 7) * 32);
}
__device__ __forceinline__ void stage_w(const u16* Wsrc, int h, const Ctx& cx, LAS unsigned char* L) {
  const int w = cx.wave, l = cx.lane;
#pragma unroll
  for (int i = 0; i < 16; ++i) {
    const int row = w * 64 + i * 4 + (l >> 4), sig = l & 15;
    const int chunk = (((sig >> 1) ^ ((row & 3) << 1)) << 1) | (sig & 1);
    GLDS(Wsrc + (size_t)row * HD + h * DH + chunk * 8, L + w * 16384 + i * 1024);
  }
}
__device__ __forceinline__ void read_wt(const Ctx& cx, LAS unsigned char* L, int nt, u32x4 (&A)[32]) {
  const int l = cx.lane, hi = l >> 5, blk = (l >> 4) & 1, q = (l & 15) >> 2, pp = l & 3;
  LAS unsigned char* b0 = L + (8 * hi + q) * 256 + (((2 * nt + blk) ^ (q << 1)) * 32) + pp * 8;
#pragma unroll
  for (int u = 0; u < 32; ++u) {
    const u32x2 lo = tr_read(b0 + u * 4096), h2 = tr_read(b0 + u * 4096 + 1024);
    A[u] = (u32x4){lo.x, lo.y, h2.x, h2.y};
  }
}
constexpr int XSTR = 1040;
__device__ __forceinline__ void xn_ready(const Ctx& cx_) {
  const Ctx cx = fresh(cx_);
  if (cx.wave == 0) {
    wait_words(cx, C_XN, 8, 12u);
    __builtin_amdgcn_fence(__ATOMIC_ACQUIRE, "agent");
    DRAIN();
  }
  BAR();
}
__device__ __forceinline__ void proj_tile(const Ctx& cx, LAS unsigned char* xt, const u32x4 (&A)[32], unsigned dst) {
  const int l = cx.lane, hi = l >> 5;
  LAS unsigned char* b = xt + (l & 31) * XSTR + hi * 16;
  f32x16 acc = {0.f};
#pragma unroll
  for (int u = 0; u < 32; ++u) acc = mfma32(A[u], *(LAS u32x4*)(b + u * 32), acc);
#pragma unroll
  for (int q = 0; q < 2; ++q) {
    u32x4 o;
    o.x = pk2(acc[8 * q + 0], acc[8 * q + 1]); o.y = pk2(acc[8 * q + 2], acc[8 * q + 3]);
    o.z = pk2(acc[8 * q + 4], acc[8 * q + 5]); o.w = pk2(acc[8 * q + 6], acc[8 * q + 7]);
    st_sc1(cx.ws, dst + (2 * q + hi) * 512 + (l & 31) * 16, o);
  }
}
__device__ __forceinline__ void mod_part(const Params& p, const Ctx& cx_, LAS unsigned char* L, int t32);
__device__ __forceinline__ void proj_role(const Params& p, const Ctx& cx, LAS unsigned char* L, int h, int kind, int j) {
  const int w = cx.wave, l = cx.lane;
  {
    u32x4 A[32];
    if (kind == 2) ln_part<2>(p, cx, 64 * h + 48, 3 * h + 2);
    else ln_part<3>(p, cx, 64 * h + 24 * kind, 3 * h + kind);

    stage_w(kind == 0 ? p.Wq : (kind == 1 ? p.Wk : p.Wv), h, cx, L);
    xn_ready(cx);
    const u16* xn = (const u16*)(p.ws + OFF_XN);
    u32x4 x0[8];
#pragma unroll
    for (int i = 0; i < 8; ++i) x0[i] = *(const GAS u32x4*)(xn + (size_t)(8 * w + i) * DM + l * 8);
    asm volatile("s_waitcnt vmcnt(8)" ::: "memory");
    BAR();
    read_wt(cx, L, w & 3, A);
    BAR();

    const size_t obase = kind == 0 ? OFF_QX : (kind == 1 ? OFF_KX : OFF_VX);
    auto dma = [&](int T, int buf) {
#pragma unroll
      for (int i = 0; i < 8; ++i) GLDS(xn + (size_t)(64 * T + 8 * w + i) * DM + l * 8, L + buf * (64 * XSTR) + (8 * w + i) * XSTR);
    };
#pragma unroll
    for (int i = 0; i < 8; ++i) *(LAS u32x4*)(L + (8 * w + i) * XSTR + l * 16) = x0[i];
    BAR();
#pragma unroll 1
    for (int T = 0; T < 32; ++T) {
      if (T + 1 < 32) dma(T + 1, (T + 1) & 1);
      const int s = 2 * T + (w >> 2);
      proj_tile(cx, L + (T & 1) * (64 * XSTR) + (w >> 2) * (32 * XSTR), A, (unsigned)(obase + ((size_t)s * NH + h) * 8192) + (w & 3) * 2048);
      DRAIN();
      BAR();
      if (w == 0 && l == 0) {
        if (kind == 2) __hip_atomic_store(cx.ctrl + C_V + h * 32, (unsigned)T, RLX_AGENT);
        else arrive(cx, C_KQ + T * 32);
      }
    }
  }
  if (kind == 2) mod_part(p, cx, L, h);
}
constexpr int U_SPLIT = 3;
__device__ __forceinline__ void u_aload(const Ctx& cx, int h, int seg, u32x4 (&A)[8]) {
  const int l = cx.lane;
#pragma unroll
  for (int u = 0; u < 8; ++u) A[u] = ld_sc1(cx.ws, (unsigned)(OFF_VX + ((size_t)seg * NH + h) * 8192) + (2 * u + (l >> 5)) * 512 + (l & 31) * 16);
}
__device__ __forceinline__ void u_seg(const Ctx& cx, int h, int seg, const u32x4 (&A)[8], const u32x4 (&B)[2][8]) {
  const int w = cx.wave, l = cx.lane, hi = l >> 5;
#pragma unroll
  for (int nt = 0; nt < 2; ++nt) {
    f32x16 acc = {0.f};
#pragma unroll
    for (int u = 0; u < 8; ++u) acc = mfma32(A[u], B[nt][u], acc);
    const int col = 64 * w + 32 * nt + (l & 31);
    const unsigned base = (unsigned)(OFF_U + ((size_t)seg * NCOL + (col >> 3)) * U_SC) + ((h * 5 + hi) * 8 + (col & 7)) * 16;
    u32x4 o;
    o.x = pk2(acc[0], acc[1]); o.y = pk2(acc[2], acc[3]); o.z = pk2(acc[8], acc[9]); o.w = pk2(acc[10], acc[11]);
    st_sc1(cx.ws, base, o);
    o.x = pk2(acc[4], acc[5]); o.y = pk2(acc[6], acc[7]); o.z = pk2(acc[12], acc[13]); o.w = pk2(acc[14], acc[15]);
    st_sc1(cx.ws, base + 256, o);
    __builtin_amdgcn_sched_barrier(0);
  }
}
__device__ __forceinline__ void u_early(const Ctx& cx, int h, int k, const u32x4 (&B)[2][8]) {
#pragma unroll 1
  for (int T = 0; T < U_SPLIT; ++T) {
    wait_words(cx, C_V + h * 32, 1, (unsigned)(T + 1));
    u32x4 A[8];
    u_aload(cx, h, 2 * T + k, A);
    u_seg(cx, h, 2 * T + k, A, B);
    DRAIN();
    BAR();
    if (cx.wave == 0 && cx.lane == 0) arrive(cx, C_U + (2 * T + k) * 32);
  }
}
__device__ __forceinline__ void u_role(const Params& p, const Ctx& cx_, LAS unsigned char* L, int h, int j) {
  u32x4 B[2][8];
  wo_issue(p, cx_, L, h);
  DRAIN();
  BAR();
  wvo_part(p, cx_, L, h, B);
  const Ctx cx = fresh(cx_);
  const int w = cx.wave, l = cx.lane;
  u_early(cx, h, 0, B);

  auto tile = [&](const u32x4 (&A)[2][8], u32x4 (&An)[2][8], int T) {
    const bool pre = (T + 1 < 32) && test_word(cx, C_V + h * 32, (unsigned)(T + 2));
    if (pre) { u_aload(cx, h, 2 * T + 2, An[0]); u_aload(cx, h, 2 * T + 3, An[1]); }
    u_seg(cx, h, 2 * T, A[0], B);
    u_seg(cx, h, 2 * T + 1, A[1], B);
    DRAIN();
    BAR();
    if (w == 0 && l < 2) arrive(cx, C_U + (2 * T + l) * 32);
    if (!pre && T + 1 < 32) { wait_words(cx, C_V + h * 32, 1, (unsigned)(T + 2)); u_aload(cx, h, 2 * T + 2, An[0]); u_aload(cx, h, 2 * T + 3, An[1]); }
  };
  u32x4 Aa[2][8], Ab[2][8];
  wait_words(cx, C_V + h * 32, 1, (unsigned)(U_SPLIT + 1));
  u_aload(cx, h, 2 * U_SPLIT, Aa[0]); u_aload(cx, h, 2 * U_SPLIT + 1, Aa[1]);
  tile(Aa, Ab, U_SPLIT);
#pragma unroll 1
  for (int T = U_SPLIT + 1; T < 32; T += 2) {
    tile(Ab, Aa, T);
    tile(Aa, Ab, T + 1);
  }
}

__device__ __forceinline__ void topk_part(const Ctx& cx, LAS int* IDX) {
  const int w = cx.wave, l = cx.lane, tid = w * 64 + l;
  LAS unsigned* CAND = (LAS unsigned*)(IDX + 16);
  unsigned key[4];
#pragma unroll
  for (int i = 0; i < 4; ++i) {
    const int row = tid + 512 * i;
    const unsigned rb = __builtin_amdgcn_raw_buffer_load_b32(cx.ws, (unsigned)(OFF_STAT + (size_t)row * 16 + 8), 0, AUX_SC1);
    const unsigned mono = (rb & 0x80000000u) ? ~rb : (rb | 0x80000000u);
    key[i] = (mono & 0xFFFF0000u) | (unsigned)(0xFFFF - row);
  }
  unsigned mine = 0;
#pragma unroll
  for (int c = 0; c < TOPC; ++c) {
    const unsigned b = wave_umax(max(max(key[0], key[1]), max(key[2], key[3])));
#pragma unroll
    for (int i = 0; i < 4; ++i) if (key[i] == b) key[i] = 0u;
    if (l == c) mine = b;
  }
  if (l < 8) CAND[w * 8 + l] = mine;
  BAR();
  unsigned k2 = CAND[l];
#pragma unroll
  for (int c = 0; c < TOPC; ++c) {
    const unsigned b = wave_umax(k2);
    if (k2 == b) k2 = 0u;
    if (l == c) mine = b;
  }
  if (w == 0 && l < 8) {
    const unsigned mono = mine & 0xFFFF0000u;
    const float r = u2f((mono & 0x80000000u) ? (mono & 0x7fffffffu) : (~mono & 0xFFFF0000u));
    IDX[l] = 0xFFFF - (int)(mine & 0xFFFFu);
    IDX[8 + l] = (int)f2u(bfr(1.f / (1.f + __expf(-r))));
  }
  BAR();
}

__device__ __forceinline__ void mod_part(const Params& p, const Ctx& cx_, LAS unsigned char* L, int t32) {
  const Ctx cx = fresh(cx_);
  const int w = cx.wave, l = cx.lane, tid = w * 64 + l, g = l >> 4, n = l & 15;
  LAS int* IDX = (LAS int*)(L + 140 * 1024);
  LAS float* UX = (LAS float*)(L + 141 * 1024);
  if (t32 < 28) {
    const u16* wsrc = (w < 4 ? p.w_gate : p.w_up) + 64 * t32 + (l & 7) * 8;
#pragma unroll
    for (int i = 0; i < 16; ++i) {
      const int blk = (w & 3) * 16 + i;
      GLDS(wsrc + (size_t)(8 * blk + (l >> 3)) * FF, L + (w >> 2) * 65536 + blk * 1024);
    }
  }
  topk_part(cx, IDX);
  DRAIN();
  BAR();
  const int q4 = n >> 2, pp = l & 3;
  if (t32 < 28) {
    const unsigned xb = (unsigned)(OFF_XN + (size_t)IDX[l & 7] * 1024) + g * 16;
    u32x4 A[16];
#pragma unroll
    for (int u = 0; u < 16; ++u) A[u] = ld_sc1(cx.ws, xb + u * 64);
    LAS unsigned char* b0 = L + (w >> 2) * 65536 + (8 * g + q4) * 128 + (w & 3) * 32 + pp * 8;
    f32x4 acc = {0.f, 0.f, 0.f, 0.f};
#pragma unroll
    for (int u = 0; u < 16; ++u) {
      const u32x2 lo = tr_read(b0 + u * 4096), h2 = tr_read(b0 + u * 4096 + 512);
      acc = mfma16(A[u], (u32x4){lo.x, lo.y, h2.x, h2.y}, acc);
    }
    if (w >= 4 && g < 2) {
#pragma unroll
      for (int r = 0; r < 4; ++r) UX[(4 * g + r) * 64 + (w & 3) * 16 + n] = bfr(acc[r]);
    }
    BAR();
    LAS u16* GT = (LAS u16*)(L + 144 * 1024);
    if (w < 4 && g < 2) {
#pragma unroll
      for (int r = 0; r < 4; ++r) {
        const float gs = bfr(acc[r]);
        const float sg = bfr(gs / (1.f + __expf(-gs)));
        GT[(4 * g + r) * 64 + w * 16 + n] = f2bf(sg * UX[(4 * g + r) * 64 + w * 16 + n]);
      }
    }
    BAR();
    if (w == 0) {
      st_sc1(cx.ws, (unsigned)(OFF_GU + (size_t)(l >> 3) * (FF * 2) + t32 * 128 + (l & 7) * 16), *(LAS u32x4*)((LAS unsigned char*)GT + l * 16));
      DRAIN();
      if (l == 0) arrive(cx, C_GU);
    }
  }
  {
    const u16* wsrc = p.w_down + 16 * t32 + (l & 1) * 8;
#pragma unroll
    for (int i = 0; i < 7; ++i) {
      const int blk = w * 7 + i;
      GLDS(wsrc + (size_t)(32 * blk + (l >> 1)) * DM, L + blk * 1024);
    }
    if (w == 0) wait_words(cx, C_GU, 1, 28u);
    DRAIN();
    BAR();
    const unsigned gb = (unsigned)(OFF_GU + (size_t)(l & 7) * (FF * 2)) + (7 * w) * 64 + g * 16;
    u32x4 A[7];
#pragma unroll
    for (int u = 0; u < 7; ++u) A[u] = ld_sc1(cx.ws, gb + u * 64);
    LAS unsigned char* b0 = L + (7 * w) * 1024 + (8 * g + q4) * 32 + pp * 8;
    f32x4 acc = {0.f, 0.f, 0.f, 0.f};
#pragma unroll
    for (int u = 0; u < 7; ++u) {
      const u32x2 lo = tr_read(b0 + u * 1024), h2 = tr_read(b0 + u * 1024 + 128);
      acc = mfma16(A[u], (u32x4){lo.x, lo.y, h2.x, h2.y}, acc);
    }
    LAS float* PR = (LAS float*)(L + 65536);
    if (g < 2) {
#pragma unroll
      for (int r = 0; r < 4; ++r) PR[(w * 8 + 4 * g + r) * 16 + n] = acc[r];
    }
    BAR();
    if (w < 2) {
      const int r = tid >> 4;
      float y = 0.f;
#pragma unroll
      for (int k = 0; k < 8; ++k) y += PR[(k * 8 + r) * 16 + n];
      st_sc1_32(cx.ws, (unsigned)(OFF_Y + (size_t)(r * DM + 16 * t32 + n) * 4), f2u(bfr(bfr(y) * u2f((unsigned)IDX[8 + r]))));
      DRAIN();
    }
    BAR();
    if (tid == 0) arrive(cx, C_Y);
  }
}

__device__ __forceinline__ void to_role(const Params& p, const Ctx& cx_, LAS unsigned char* L, int x, int f) {
  const int t32 = x * 8 + f;
  {
    u32x4 B[2][8];
    wo_issue(p, cx_, L, 32 + t32);
    DRAIN();
    BAR();
    wvo_part(p, cx_, L, 32 + t32, B);
    u_early(fresh(cx_), t32, 1, B);
  }
  const Ctx cx = fresh(cx_);
  const int w = cx.wave, l = cx.lane, tid = w * 64 + l, hi = l >> 5;

  LAS int* IDX = (LAS int*)(L + 140 * 1024);
  LAS f32x4* SELT = (LAS f32x4*)(L + 141 * 1024);
  if (w == 0) { wait_words(cx, C_XN, 8, 12u); wait_words(cx, C_RDY, 8, 12u); }
  BAR();
  const unsigned pol = get_pol(cx);
  topk_part(cx, IDX);

  const int hf = f & 1;
  const int cb0 = x * 16 + hf * 8;
  const int ocol = 128 * x + 64 * hf + (tid & 15) * 4;
  LAS float* RD = (LAS float*)L;
#pragma unroll 1
  for (int s = f >> 1; s < NSEG; s += 4) {
    const bool last = s + 4 >= NSEG;
    if (w == 0) { wait_words(cx, C_U + s * 32, 1, 32u); wait_words(cx, C_KQ + (s >> 1) * 32, 1, 64u); if (last) wait_words(cx, C_Y, 1, 32u); }
    BAR();
    if (last && tid < 128) {
      const int sl = tid >> 4, row = IDX[sl];
      if (((row >> 5) & 3) == (f >> 1) && (row >> 5) < s) {
        const f32x4 y4 = __builtin_bit_cast(f32x4, ld_sc1(cx.ws, (unsigned)(OFF_Y + (size_t)(sl * DM + ocol) * 4)));
        const f32x4 t1 = SELT[tid * 2], xn4 = SELT[tid * 2 + 1];
        u32x2 ov; ov.x = pk2(t1[0] + bfr(xn4[0] + y4[0]), t1[1] + bfr(xn4[1] + y4[1])); ov.y = pk2(t1[2] + bfr(xn4[2] + y4[2]), t1[3] + bfr(xn4[3] + y4[3]));
        *(GAS u32x2*)(p.out + (size_t)row * DM + ocol) = ov;
      }
    }
    const unsigned ub = (unsigned)(OFF_U + ((size_t)s * NCOL + cb0 + ((l & 31) >> 3)) * U_SC) + (l & 7) * 16 + w * (4 * 5 * 128);
    u32x4 Bx[2][8];
#pragma unroll
    for (int nt = 0; nt < 2; ++nt)
#pragma unroll
      for (int u = 0; u < 8; ++u) Bx[nt][u] = ld_sc1(cx.ws, ub + nt * (4 * (unsigned)U_SC) + ((u >> 1) * 5 + 2 * (u & 1) + hi) * 128);
    {
      u32x4 t[8];
      const unsigned kb = (unsigned)(((size_t)s * NH + x * 8 + hf * 4) * 8192) + tid * 16;
#pragma unroll
      for (int i = 0; i < 4; ++i) { t[i] = ld_sc1(cx.ws, (unsigned)OFF_KX + kb + i * 8192); t[4 + i] = ld_sc1(cx.ws, (unsigned)OFF_QX + kb + i * 8192); }
#pragma unroll
      for (int i = 0; i < 8; ++i) asm volatile("" ::"v"(t[i]));
    }
    const int orow = 32 * s + (tid >> 4);
    const u32x2 xin = *(const GAS u32x2*)(p.x + (size_t)orow * DM + ocol);
    const u32x2 stat = __builtin_amdgcn_raw_buffer_load_b64(cx.ws, (unsigned)(OFF_STAT + (size_t)orow * 16), 0, AUX_SC1);
    int sel = -1;
#pragma unroll
    for (int c = 0; c < TOPC; ++c) if (IDX[c] == orow) sel = c;
    f32x4 y4 = {0.f, 0.f, 0.f, 0.f};
    if (last && sel >= 0) y4 = __builtin_bit_cast(f32x4, ld_sc1(cx.ws, (unsigned)(OFF_Y + (size_t)(sel * DM + ocol) * 4)));
    if (w == 0) {
      const unsigned fa = ex_addr(l < 32 ? FPU + l * 128 : FUMU + (cb0 + (l & 7)) * 128, pol) + rep_off(0, pol);
      for (unsigned spins = 0;; ++spins) {
        asm volatile("" ::: "memory");
        const unsigned v = __builtin_amdgcn_raw_buffer_load_b32(cx.ex, fa, 0, AUX_SC1) + 1u;
        if (__all(v >= (unsigned)(s + 1))) break;
        if (spins > WAIT_CAP) { __hip_atomic_store(cx.ctrl + C_TMO, 1u, RLX_AGENT); break; }
        if ((spins & 63u) == 63u && __builtin_amdgcn_readfirstlane(__hip_atomic_load(cx.ctrl + C_TMO, RLX_AGENT)) == 1u) break;
        __builtin_amdgcn_s_sleep(TO_SLEEP);
      }
    }
    BAR();
    const unsigned pb = (unsigned)(OFF_PX + ((size_t)s * NH + w * 4) * 2560) + (l & 31) * 16;
    u32x4 Ax[8], Am[2], Bm[2][2];
#pragma unroll
    for (int u = 0; u < 8; ++u) Ax[u] = ld_sc1(cx.ws, pb + ((u >> 1) * 5 + 2 * (u & 1) + hi) * 512);
#pragma unroll
    for (int v = 0; v < 2; ++v) {
      Am[v] = ld_sc1(cx.ws, pb + ((2 * v + hi) * 5 + 4) * 512);
      Bm[0][v] = ld_sc1(cx.ws, ub + ((2 * v + hi) * 5 + 4) * 128);
      Bm[1][v] = ld_sc1(cx.ws, ub + 4 * (unsigned)U_SC + ((2 * v + hi) * 5 + 4) * 128);
    }
#pragma unroll
    for (int nt = 0; nt < 2; ++nt) {
      f32x16 acc = {0.f};
#pragma unroll
      for (int u = 0; u < 8; ++u) acc = mfma32(Ax[u], Bx[nt][u], acc);
#pragma unroll
      for (int v = 0; v < 2; ++v) acc = mfma32(Am[v], Bm[nt][v], acc);
#pragma unroll
      for (int r = 0; r < 16; ++r) RD[(w * 32 + (r & 3) + 8 * (r >> 2) + 4 * hi) * 68 + nt * 32 + (l & 31)] = acc[r];
    }
    BAR();
    {
      LAS float* q = RD + (tid >> 4) * 68 + (tid & 15) * 4;
      f32x4 sum = *(LAS f32x4*)q;
#pragma unroll
      for (int k = 1; k < 8; ++k) sum += *(LAS f32x4*)(q + k * 32 * 68);
      const float mb = u2f(stat.x), den = u2f(stat.y);
      f32x4 t1, xn4;
#pragma unroll
      for (int k = 0; k < 4; ++k) {
        const float xv = (k & 1) ? bfhi(xin[k >> 1]) : bflo(xin[k >> 1]);
        xn4[k] = bfr(bfr(xv - mb) / den);
        t1[k] = bfr(xv + bfr(sum[k]));
      }
      if (sel >= 0 && !last) { SELT[(sel * 16 + (tid & 15)) * 2] = t1; SELT[(sel * 16 + (tid & 15)) * 2 + 1] = xn4; }
      else {
        u32x2 ov; ov.x = pk2(t1[0] + bfr(xn4[0] + y4[0]), t1[1] + bfr(xn4[1] + y4[1])); ov.y = pk2(t1[2] + bfr(xn4[2] + y4[2]), t1[3] + bfr(xn4[3] + y4[3]));
        *(GAS u32x2*)(p.out + (size_t)orow * DM + ocol) = ov;
      }
    }
  }
}

constexpr int LDS_BYTES = 160 * 1024;

__global__ void __launch_bounds__(512, 2) k_fused(Params p) {
  extern __shared__ __attribute__((aligned(16))) unsigned char lds_raw[];
  LAS unsigned char* L = (LAS unsigned char*)lds_raw;
  const int tid = threadIdx.x;
  Ctx cx;
  cx.wave = __builtin_amdgcn_readfirstlane(tid >> 6); cx.lane = tid & 63;
  cx.ctrl = (gu32*)p.ws;
  const uintptr_t exb = ((uintptr_t)p.ws + CTRL_BYTES + 524287) & ~(uintptr_t)524287;
  cx.ex = __builtin_amdgcn_make_buffer_rsrc((void*)exb, 0, (int)EX_BYTES, 0x00020000);
  cx.exs = (u32x4){(unsigned)exb, (unsigned)(exb >> 32) & 0xffffu, (unsigned)EX_BYTES, 0x00020000u};
  cx.ws = __builtin_amdgcn_make_buffer_rsrc((void*)p.ws, 0, (int)WS_END, 0x00020000);
  cx.pol = 0;
  volatile LAS int* sh = (volatile LAS int*)(L + 155 * 1024);
  if (tid == 0) {
    const unsigned x = xcc_id() & 7u;
    const unsigned t = __hip_atomic_fetch_add(cx.ctrl + C_RC + x * 32, 1u, RLX_AGENT) + 1u;
    __hip_atomic_fetch_add(cx.ctrl + C_TOT, 1u, RLX_AGENT);
    int role;
    if (t < 32u) role = (int)(x * 32 + t);
    else {
      const unsigned o = __hip_atomic_fetch_add(cx.ctrl + C_OVF, 1u, RLX_AGENT) + 1u;
      unsigned spins = 0; role = -1;
      while (__hip_atomic_load(cx.ctrl + C_TOT, RLX_AGENT) + 1u < 256u) { if (++spins > WAIT_CAP) break; __builtin_amdgcn_s_sleep(2); }
      unsigned seen = 0;
      for (int xx = 0; xx < 8 && role < 0; ++xx) {
        const unsigned cnt = __hip_atomic_load(cx.ctrl + C_RC + xx * 32, RLX_AGENT) + 1u;
        const unsigned holes = cnt < 32u ? 32u - cnt : 0u;
        if (o < seen + holes) role = xx * 32 + (int)(cnt + (o - seen));
        seen += holes;
      }
    }
    sh[0] = role;
  }
  __syncthreads();
  const int role = __builtin_amdgcn_readfirstlane(sh[0]);
  __syncthreads();
  if (role < 0) return;

  const int rx = role >> 5, rt_ = role & 31;
  if (rx >= 4) {
    const int j = role - 128, h = j >> 2, k = j & 3;
    if (k < 3) proj_role(p, cx, L, h, k, j);
    else u_role(p, cx, L, h, j);

    return;
  }
  if (rt_ >= 24) { to_role(p, cx, L, rx, rt_ - 24); return; }
  if (rt_ == 0) {
    if (cx.wave == 0) {
      unsigned best[4] = {~0u, ~0u, ~0u, ~0u};
#pragma unroll 1
      for (int rep = 0; rep < 3; ++rep) {
#pragma unroll
        for (int c = 0; c < 4; ++c) {
          const unsigned t0 = now32();
          if (cx.lane == 0) st_sc1_32(cx.ex, ex_addr(FPU + (60 + rx) * 128, c) + rep_off(0, c), 0u);
          DRAIN();
          best[c] = min(best[c], now32() - t0);
        }
      }
      unsigned pk = 0;
#pragma unroll
      for (int c = 0; c < 4; ++c) pk |= min(best[c] >> 3, 127u) << (8 * c);
      if (cx.lane == 0) __hip_atomic_store(cx.ctrl + C_POL + rx * 32, pk, RLX_AGENT);
    }
    __syncthreads();
  }
  if (rt_ < 8) head_role(p, cx, L, rx * 8 + rt_);
  else col_role(p, cx, L, rx * 16 + rt_ - 8);
}

extern "C" void kernel_launch(void* const* d_in, const int* in_sizes, int n_in, void* d_out, int out_size, void* d_ws, size_t ws_size, hipStream_t stream) {
  Params p;
  p.x = (const u16*)d_in[0]; p.mem0 = (const u16*)d_in[1]; p.Wq = (const u16*)d_in[2]; p.Wk = (const u16*)d_in[3]; p.Wv = (const u16*)d_in[4]; p.Wo = (const u16*)d_in[5];
  p.w_r = (const u16*)d_in[8]; p.w_gate = (const u16*)d_in[9]; p.w_up = (const u16*)d_in[10]; p.w_down = (const u16*)d_in[11];
  p.out = (u16*)d_out; p.ws = (unsigned char*)d_ws;
  p.dh = D_H; p.dm = D_M; p.dp = D_P; p.pad = 0;
  (void)hipFuncSetAttribute((const void*)k_fused, hipFuncAttributeMaxDynamicSharedMemorySize, LDS_BYTES);
  (void)hipMemsetAsync(d_ws, 0xFF, CTRL_BYTES, stream);
  k_fused<<<256, 512, LDS_BYTES, stream>>>(p);
}
```
